```python
import torch

def _softmax(x, axis=-1):
    return torch.softmax(x, dim=axis)
def _gelu(x):
    return torch.nn.functional.gelu(x, approximate="tanh")
def _silu(x):
    return torch.nn.functional.silu(x)
def _sigmoid(x):
    return torch.sigmoid(x)
def _layernorm(x, axis=-1, eps=1e-5):
    m = x.mean(dim=axis, keepdim=True)
    v = x.var(dim=axis, keepdim=True, unbiased=False)
    return (x - m) / torch.sqrt(v + eps)
def _scatter_add(tgt, idx, src, axis=0):
    return tgt.clone().index_add_(axis, idx, src)
def _segment_sum(src, seg, n):
    out = torch.zeros((n,) + src.shape[1:], dtype=src.dtype)
    return out.index_add_(0, seg, src)
def _pad(x, pw):
    flat = tuple(v for pair in reversed(pw) for v in pair)
    return torch.nn.functional.pad(x, flat)

kernel_name = 'recurrent_scan__gqa_block_cached__glu_gated_bilinear__decode'

def setup_inputs():

    _w = (lambda a: (lambda t: t.to(torch.bfloat16) if t.is_floating_point() else t)(torch.as_tensor(a)))
    def _A_setup():
        B, Sq, D = 2, 1, 640
        Hq, Hkv, Sk, Dh = 32, 32, 4096, 192
        assert Hq % Hkv == 0, (Hq, Hkv)
        return {
            "x": _w((lambda *s: torch.randn(*s, dtype=torch.bfloat16) * 0.2)(B, Sq, D)),
            "k_cache": _w((lambda *s: torch.randn(*s, dtype=torch.bfloat16) * 1.6)(B, Hkv, Sk, Dh)),
            "v_cache": _w((lambda *s: torch.randn(*s, dtype=torch.bfloat16) * 1.6)(B, Hkv, Sk, Dh)),
            "wq": _w((lambda *s: torch.randn(*s, dtype=torch.bfloat16) * 1.6)(D, Hq * Dh) * (D ** -0.5)),
            "wo": _w((lambda *s: torch.randn(*s, dtype=torch.bfloat16) * 1.6)(Hq * Dh, D) * ((Hq * Dh) ** -0.5)),
        }
    _A_in = _A_setup()

    def _B_setup():
        B, S, D, F = 2, 1, 640, 1536
        return {
            "x": _w((lambda *s: torch.randn(*s, dtype=torch.bfloat16) * 0.2)(B, S, D)),
            "w_gate": _w((lambda *s: torch.randn(*s, dtype=torch.bfloat16) * 0.8)(D, F) * (D ** -0.5)),
            "w_up": _w((lambda *s: torch.randn(*s, dtype=torch.bfloat16) * 0.8)(D, F) * (D ** -0.5)),
            "w_down": _w((lambda *s: torch.randn(*s, dtype=torch.bfloat16) * 0.8)(F, D) * (F ** -0.5)),
        }
    _B_in = _B_setup()

    return {
        "A_x": _A_in["x"],
        "A_k_cache": _A_in["k_cache"],
        "A_v_cache": _A_in["v_cache"],
        "A_wq": _A_in["wq"],
        "A_wo": _A_in["wo"],
        "B_x": _B_in["x"],
        "B_w_gate": _B_in["w_gate"],
        "B_w_up": _B_in["w_up"],
        "B_w_down": _B_in["w_down"],
    }

def reference(A_x, A_k_cache, A_v_cache, A_wq, A_wo, B_x, B_w_gate, B_w_up, B_w_down):
    def _A_ref(x, k_cache, v_cache, wq, wo):
        B, Sq, D = x.shape
        Hq, Dh = 32, 192
        Hkv = k_cache.shape[1]
        rep = Hq // Hkv
        q = (lambda x, *d: x.permute(*d))((x @ wq).reshape(B, Sq, Hq, Dh), 0, 2, 1, 3)
        k = (lambda x, n, axis: torch.repeat_interleave(x, n, dim=axis))(k_cache, rep, axis=1)
        v = (lambda x, n, axis: torch.repeat_interleave(x, n, dim=axis))(v_cache, rep, axis=1)
        scores = torch.einsum("bhqd,bhkd->bhqk", q, k) * (Dh ** -0.5)
        p = _softmax(scores, axis=-1)
        o = torch.einsum("bhqk,bhkd->bhqd", p, v)
        return (lambda x, *d: x.permute(*d))(o, 0, 2, 1, 3).reshape(B, Sq, Hq * Dh) @ wo

    def _B_ref(x, w_gate, w_up, w_down):
        g = torch.einsum("bsd,df->bsf", x, w_gate)
        u = torch.einsum("bsd,df->bsf", x, w_up)
        h = g * u
        return torch.einsum("bsf,fd->bsd", h, w_down)

    x = A_x
    xn = _layernorm(x, axis=-1)
    s = _A_ref(xn, A_k_cache, A_v_cache, A_wq, A_wo)
    g = _silu(_B_ref(xn, B_w_gate, B_w_up, B_w_down))
    return x + s * g
```

```cpp
#include <hip/hip_runtime.h>
#include <hip/hip_bf16.h>
#include <stdint.h>

#define MIMIC 1

constexpr int B = 2, D = 640, H = 32, SK = 4096, DH = 192, F = 1536;
constexpr int NSPLIT = 8;
typedef unsigned u32x4 __attribute__((ext_vector_type(4)));
typedef __bf16 bf16x2 __attribute__((ext_vector_type(2)));

__device__ __forceinline__ float bf2f(unsigned short v) { return __uint_as_float(((unsigned)v) << 16); }
__device__ __forceinline__ float rbf(float f) { return (float)(__bf16)f; }
__device__ __forceinline__ float dot2(unsigned k, unsigned q, float acc) {
  return __builtin_amdgcn_fdot2_f32_bf16(__builtin_bit_cast(bf16x2, k), __builtin_bit_cast(bf16x2, q), acc, false);
}

__device__ void layernorm(const unsigned short* x, float* xn  , float* red) {
  int tid = threadIdx.x, lane = tid & 63, w = tid >> 6;
  if (w < 2) {
    float v[10]; float s = 0.f;
    for (int i = 0; i < 10; ++i) { v[i] = bf2f(x[w * D + i * 64 + lane]); s += v[i]; }
    for (int o = 32; o; o >>= 1) s += __shfl_xor(s, o);
    float mean = s / D; float q = 0.f;
    for (int i = 0; i < 10; ++i) { float d = v[i] - mean; q += d * d; }
    for (int o = 32; o; o >>= 1) q += __shfl_xor(q, o);
    float var = q / D;
    float mb = rbf(mean), vb = rbf(var);
    float sq = rbf(sqrtf(rbf(vb + 1e-5f)));
    for (int i = 0; i < 10; ++i) xn[w * D + i * 64 + lane] = rbf(rbf(v[i] - mb) / sq);
  }
  __syncthreads();
}

__global__ void __launch_bounds__(256) k_pre(const unsigned short* x, const unsigned short* wq, const unsigned short* wg, const unsigned short* wu, float* q, float* hbuf) {
  __shared__ float xn[2 * D];
  __shared__ float red[4][4][64];
  layernorm(x, xn, nullptr);
  int tid = threadIdx.x, lane = tid & 63, w = tid >> 6;
  int blk = blockIdx.x;
  if (blk < 96) {
    int col = blk * 64 + lane; float a0 = 0, a1 = 0;
    for (int d = w * 160; d < w * 160 + 160; ++d) { float wv = bf2f(wq[(size_t)d * (H * DH) + col]); a0 += xn[d] * wv; a1 += xn[D + d] * wv; }
    red[w][0][lane] = a0; red[w][1][lane] = a1;
    __syncthreads();
    if (w < 2) { float s = red[0][w][lane] + red[1][w][lane] + red[2][w][lane] + red[3][w][lane]; q[w * (H * DH) + col] = rbf(s); }
  } else {
    int col = (blk - 96) * 64 + lane; float g0 = 0, g1 = 0, u0 = 0, u1 = 0;
    for (int d = w * 160; d < w * 160 + 160; ++d) { float a = bf2f(wg[(size_t)d * F + col]), b = bf2f(wu[(size_t)d * F + col]); g0 += xn[d] * a; g1 += xn[D + d] * a; u0 += xn[d] * b; u1 += xn[D + d] * b; }
    red[w][0][lane] = g0; red[w][1][lane] = g1; red[w][2][lane] = u0; red[w][3][lane] = u1;
    __syncthreads();
    if (w < 2) { float g = red[0][w][lane] + red[1][w][lane] + red[2][w][lane] + red[3][w][lane];
      float u = red[0][2 + w][lane] + red[1][2 + w][lane] + red[2][2 + w][lane] + red[3][2 + w][lane];
      hbuf[w * F + col] = rbf(rbf(g) * rbf(u));
    }
  }
}

__global__ void __launch_bounds__(256) k_attn(const float* q, const unsigned short* K, const unsigned short* V, float* pml, float* po) {
  constexpr int KEYS = SK / NSPLIT;
  __shared__ float sc[KEYS];
  __shared__ float red[4][DH];
  __shared__ float wm[4], wl[4];
  int s = blockIdx.x, bh = blockIdx.y, tid = threadIdx.x, lane = tid & 63, w = tid >> 6;
  int r = lane >> 3, c = lane & 7;
  int b = bh / H, h = bh % H;
  unsigned qp[12];
  for (int i = 0; i < 3; ++i) for (int j = 0; j < 4; ++j) {
    int d = i * 64 + c * 8 + j * 2;
    float lo = q[b * H * DH + h * DH + d], hi = q[b * H * DH + h * DH + d + 1];
    qp[i * 4 + j] = (__float_as_uint(lo) >> 16) | (__float_as_uint(hi) & 0xffff0000u);
  }
  const unsigned short* kb = K + ((size_t)bh * SK + (size_t)s * KEYS) * DH;
  const unsigned short* vb = V + ((size_t)bh * SK + (size_t)s * KEYS) * DH;
  float m = -INFINITY;
#pragma unroll 4
  for (int g = w; g < KEYS / 8; g += 4) {
    int row = g * 8 + r; float a = 0.f;
    for (int i = 0; i < 3; ++i) { u32x4 kv = __builtin_nontemporal_load((const u32x4*)(kb + (size_t)row * DH + i * 64 + c * 8));
      for (int j = 0; j < 4; ++j) a = dot2(kv[j], qp[i * 4 + j], a); }
    a += __shfl_xor(a, 1); a += __shfl_xor(a, 2); a += __shfl_xor(a, 4);
    a = rbf(rbf(a) * 0.07216878364870322f);
    if (c == 0) sc[row] = a;
    m = fmaxf(m, a);
  }
  for (int o = 32; o; o >>= 1) m = fmaxf(m, __shfl_xor(m, o));
  if (lane == 0) wm[w] = m;
  __syncthreads();
  m = fmaxf(fmaxf(wm[0], wm[1]), fmaxf(wm[2], wm[3]));
  float acc[24]; for (int i = 0; i < 24; ++i) acc[i] = 0.f;
  float l = 0.f;
#pragma unroll 4
  for (int g = w; g < KEYS / 8; g += 4) {
    int row = g * 8 + r; float p = __expf(sc[row] - m); l += p;
    for (int i = 0; i < 3; ++i) { u32x4 vv = __builtin_nontemporal_load((const u32x4*)(vb + (size_t)row * DH + i * 64 + c * 8));
      for (int j = 0; j < 4; ++j) { acc[i * 8 + j * 2] += p * __uint_as_float(vv[j] << 16); acc[i * 8 + j * 2 + 1] += p * __uint_as_float(vv[j] & 0xffff0000u); } }
  }
  for (int i = 0; i < 24; ++i) { acc[i] += __shfl_xor(acc[i], 8); acc[i] += __shfl_xor(acc[i], 16); acc[i] += __shfl_xor(acc[i], 32); }
  l += __shfl_xor(l, 8); l += __shfl_xor(l, 16); l += __shfl_xor(l, 32);
  if (r == 0) for (int i = 0; i < 3; ++i) for (int j = 0; j < 8; ++j) red[w][i * 64 + c * 8 + j] = acc[i * 8 + j];
  if (lane == 0) wl[w] = l;
  __syncthreads();
  if (tid < DH) po[((size_t)bh * NSPLIT + s) * DH + tid] = red[0][tid] + red[1][tid] + red[2][tid] + red[3][tid];
  if (tid == 0) { pml[(bh * NSPLIT + s) * 2] = m; pml[(bh * NSPLIT + s) * 2 + 1] = wl[0] + wl[1] + wl[2] + wl[3]; }
}

__global__ void __launch_bounds__(1024) k_post(const unsigned short* x, const float* pml, const float* po, const unsigned short* wo, const float* hbuf, const unsigned short* wd, __hip_bfloat16* out) {
  __shared__ float o[2 * H * DH];
  __shared__ float red[16][4][64];
  int tid = threadIdx.x, lane = tid & 63, w = tid >> 6;
  for (int i = tid; i < 2 * H * DH; i += 1024) {
    int bh = i / DH, d = i % DH; float M = -INFINITY;
    for (int s = 0; s < NSPLIT; ++s) M = fmaxf(M, pml[(bh * NSPLIT + s) * 2]);
    float L = 0, a = 0;
    for (int s = 0; s < NSPLIT; ++s) { float wt = __expf(pml[(bh * NSPLIT + s) * 2] - M); L += wt * pml[(bh * NSPLIT + s) * 2 + 1]; a += wt * po[((size_t)bh * NSPLIT + s) * DH + d]; }
    o[i] = rbf(a / L);
  }
  __syncthreads();
  int col = blockIdx.x * 64 + lane;
  float s0 = 0, s1 = 0, y0 = 0, y1 = 0;
  for (int k = w * 384; k < w * 384 + 384; ++k) { float wv = bf2f(wo[(size_t)k * D + col]); s0 += o[k] * wv; s1 += o[H * DH + k] * wv; }
  for (int k = w * 96; k < w * 96 + 96; ++k) { float wv = bf2f(wd[(size_t)k * D + col]); y0 += hbuf[k] * wv; y1 += hbuf[F + k] * wv; }
  red[w][0][lane] = s0; red[w][1][lane] = s1; red[w][2][lane] = y0; red[w][3][lane] = y1;
  __syncthreads();
  if (w < 2) { float s = 0, y = 0; for (int i = 0; i < 16; ++i) { s += red[i][w][lane]; y += red[i][2 + w][lane]; }
    s = rbf(s); y = rbf(y); float g = rbf(y / (1.f + __expf(-y))); float r = bf2f(x[w * D + col]) + rbf(s * g);
    out[w * D + col] = (__hip_bfloat16)r;
  }
}

extern "C" void kernel_launch(void* const* d_in, const int* in_sizes, int n_in, void* d_out, int out_size, void* d_ws, size_t ws_size, hipStream_t stream) {
  const unsigned short* x = (const unsigned short*)d_in[0];
  const unsigned short* K = (const unsigned short*)d_in[1];
  const unsigned short* V = (const unsigned short*)d_in[2];
  const unsigned short* wq = (const unsigned short*)d_in[3];
  const unsigned short* wo = (const unsigned short*)d_in[4];
  const unsigned short* wg = (const unsigned short*)d_in[6];
  const unsigned short* wu = (const unsigned short*)d_in[7];
  const unsigned short* wd = (const unsigned short*)d_in[8];
  float* ws = (float*)d_ws;
  float* q = ws; float* hbuf = q + 2 * H * DH; float* pml = hbuf + 2 * F; float* po = pml + 64 * NSPLIT * 2;
  k_pre<<<96 + 24, 256, 0, stream>>>(x, wq, wg, wu, q, hbuf);
  k_attn<<<dim3(NSPLIT, 64), 256, 0, stream>>>(q, K, V, pml, po);
  k_post<<<10, 1024, 0, stream>>>(x, pml, po, wo, hbuf, wd, (__hip_bfloat16*)d_out);
}
```

```cpp
#include <hip/hip_runtime.h>
#include <hip/hip_bf16.h>
#include <stdint.h>

#define AUX_KV 2
#define AUX_W 0
#define R0 5
#define DEPTH 3
#define KEARLY 3
#define GLU_AT 2
#define WD_AT 17
#define WO_AT 29

namespace {
constexpr int D = 640, H = 32, SK = 4096, DH = 192, F = 1536;
constexpr int NW = 8;
typedef unsigned u32;
typedef unsigned long long u64;
typedef u32 u32x2 __attribute__((ext_vector_type(2)));
typedef u32 u32x4 __attribute__((ext_vector_type(4)));
typedef float f32x4 __attribute__((ext_vector_type(4)));
typedef __bf16 bf16x2 __attribute__((ext_vector_type(2)));
typedef __bf16 bf16x8 __attribute__((ext_vector_type(8)));
typedef __amdgpu_buffer_rsrc_t rsrc_t;
#define SB() __builtin_amdgcn_sched_barrier(0)

constexpr u32 WS_EPOCH = 0;
constexpr u32 WS_G1 = 4096;
constexpr u32 WS_G2S = WS_G1 + 256 * 256 * 8;
constexpr u32 WS_G2Y = WS_G2S + 2 * 32 * 640 * 8;
constexpr u32 WS_END = WS_G2Y + 2 * 32 * 640 * 8;
constexpr u32 WS_STAMP = 2u << 20;
constexpr u32 SPIN_CAP = 1u << 20;

__device__ __forceinline__ rsrc_t mk(const void* p, u32 bytes) { return __builtin_amdgcn_make_buffer_rsrc(const_cast<void*>(p), 0, (int)bytes, 0x00020000); }
__device__ __forceinline__ float dot2(u32 a, u32 b, float c) { return __builtin_amdgcn_fdot2_f32_bf16(__builtin_bit_cast(bf16x2, a), __builtin_bit_cast(bf16x2, b), c, false); }
__device__ __forceinline__ f32x4 mfma(u32x4 a, u32x4 b, f32x4 c) { return __builtin_amdgcn_mfma_f32_16x16x32_bf16(__builtin_bit_cast(bf16x8, a), __builtin_bit_cast(bf16x8, b), c, 0, 0, 0); }
template <int C> __device__ __forceinline__ float dpp(float x) { return __builtin_bit_cast(float, __builtin_amdgcn_mov_dpp(__builtin_bit_cast(int, x), C, 0xf, 0xf, true)); }
constexpr int XOR1 = 0xB1, XOR2 = 0x4E, HMIR = 0x141, ROR8 = 0x128;
__device__ __forceinline__ float lo2f(u32 v) { return __uint_as_float(v << 16); }
__device__ __forceinline__ float hi2f(u32 v) { return __uint_as_float(v & 0xffff0000u); }
__device__ __forceinline__ float rbf(float f) { return (float)(__bf16)f; }
__device__ __forceinline__ u32 dupbf(float f) { bf16x2 v = {(__bf16)f, (__bf16)f}; return __builtin_bit_cast(u32, v); }
__device__ __forceinline__ float x16_32_sum(float x) {
  auto s = __builtin_amdgcn_permlane16_swap(__float_as_uint(x), __float_as_uint(x), false, false);
  x = __uint_as_float(s[0]) + __uint_as_float(s[1]);
  auto t = __builtin_amdgcn_permlane32_swap(__float_as_uint(x), __float_as_uint(x), false, false);
  return __uint_as_float(t[0]) + __uint_as_float(t[1]);
}
__device__ __forceinline__ float x16_32_max(float x) {
  auto s = __builtin_amdgcn_permlane16_swap(__float_as_uint(x), __float_as_uint(x), false, false);
  x = fmaxf(__uint_as_float(s[0]), __uint_as_float(s[1]));
  auto t = __builtin_amdgcn_permlane32_swap(__float_as_uint(x), __float_as_uint(x), false, false);
  return fmaxf(__uint_as_float(t[0]), __uint_as_float(t[1]));
}
__device__ __forceinline__ float wave_sum(float x) { x += dpp<XOR1>(x); x += dpp<XOR2>(x); x += dpp<HMIR>(x); x += dpp<ROR8>(x); return x16_32_sum(x); }
__device__ __forceinline__ void barrier() { asm volatile("s_waitcnt lgkmcnt(0)\n\ts_barrier" ::: "memory"); }
__device__ __forceinline__ u32x4 amask(u32 dup, const u32 (&m)[4]) { return u32x4{dup & m[0], dup & m[1], dup & m[2], dup & m[3]}; }

typedef __attribute__((address_space(1))) u64 gu64;
typedef __attribute__((address_space(1))) u32 gu32;
__device__ __forceinline__ void st_gran(char* ws, u32 off, float v, u32 tag) {
  __hip_atomic_store((gu64*)(ws + off), ((u64)tag << 32) | __float_as_uint(v), __ATOMIC_RELAXED, __HIP_MEMORY_SCOPE_AGENT);
}
__device__ __forceinline__ u64 ld_gran(char* ws, u32 off) { return __hip_atomic_load((gu64*)(ws + off), __ATOMIC_RELAXED, __HIP_MEMORY_SCOPE_AGENT); }
__device__ __forceinline__ float gval(u64 g) { return __uint_as_float((u32)g); }
__device__ __forceinline__ u32 gtag(u64 g) { return (u32)(g >> 32); }

struct Lds {
  float qpart[16][192];
  float opart[16][192];
  float spart[16][192];
  float gupart[16][128];
  float ml[8][2];
  u32 qbf[96];
  u32 obf[192];
  u32 hbf[64];
};

#define TS(i) do { } while (0)

__global__ void __launch_bounds__(NW * 64, 2) fused_decode(const char* __restrict__ x, const char* __restrict__ K, const char* __restrict__ V,
    const char* __restrict__ wq, const char* __restrict__ wo, const char* __restrict__ wg, const char* __restrict__ wu,
    const char* __restrict__ wd, char* __restrict__ out, char* ws) {
  __shared__ __attribute__((aligned(16))) Lds L;
  TS(0);
  asm volatile("" :: "s"(x), "s"(K), "s"(V), "s"(wq), "s"(wo), "s"(wg), "s"(wu), "s"(wd), "s"(out), "s"(ws));
  const int tid = threadIdx.x, lane = tid & 63, w = __builtin_amdgcn_readfirstlane(tid >> 6);
  const int r = lane >> 3, c = lane & 7;
  const int xl = blockIdx.x & 7, rk = blockIdx.x >> 3;
  const int h = xl * 4 + (rk >> 3), b = (rk >> 2) & 1, s = rk & 3;
  const int wgid = (b * H + h) * 4 + s;

  const rsrc_t rx = mk(x + b * (D * 2), D * 2);
  const rsrc_t rq = mk(wq + h * (DH * 2), D * H * DH * 2 - h * (DH * 2));
  const rsrc_t rK = mk(K + (size_t)wgid * (1024 * DH * 2), 1024 * DH * 2);
  const rsrc_t rV = mk(V + (size_t)wgid * (1024 * DH * 2), 1024 * DH * 2);

  const u32x4 xa = __builtin_amdgcn_raw_buffer_load_b128(rx, lane * 16, 0, 0);
  const u32x4 xb = __builtin_amdgcn_raw_buffer_load_b128(rx, lane * 16 + 1024, 0, 0);
  const u32x4 xn4 = __builtin_amdgcn_raw_buffer_load_b128(rx, r * 20, w * 160, 0);
  const u32 xn1 = __builtin_amdgcn_raw_buffer_load_b32(rx, r * 20 + 16, w * 160, 0);
  const u32 epoch = __hip_atomic_load((gu32*)(ws + WS_EPOCH + b * 256), __ATOMIC_RELAXED, __HIP_MEMORY_SCOPE_AGENT);
  SB();
  const int kvoff = r * 384 + c * 16;
  u32x4 kv[32][3];
  auto issue_kv = [&](int j) {
#pragma unroll
    for (int i = 0; i < 3; ++i) kv[j][i] = __builtin_amdgcn_raw_buffer_load_b128(j < 16 ? rK : rV, kvoff + i * 128, ((j & 15) * NW + w) * 3072, AUX_KV);
    SB();
  };
  u32x4 wqr[10][3];
  const int wqoff = r * (10 * H * DH * 2) + c * 16;
#pragma unroll
  for (int g = 0; g < 10; ++g) {
    if (g == KEARLY) issue_kv(0);
#pragma unroll
    for (int i = 0; i < 3; ++i) wqr[g][i] = __builtin_amdgcn_raw_buffer_load_b128(rq, wqoff + i * 128, (w * 80 + g) * (H * DH * 2), AUX_W);
    SB();
  }
#pragma unroll
  for (int j = (KEARLY < 10 ? 1 : 0); j < R0; ++j) issue_kv(j);
  TS(1);
  const u32 tag = __builtin_amdgcn_readfirstlane(epoch) + 0x9E3779B9u;

  u32 mask[4];
#pragma unroll
  for (int t = 0; t < 4; ++t) mask[t] = ((c >> 1) == t) ? ((c & 1) ? 0xffff0000u : 0x0000ffffu) : 0u;
  u32 xdup[10];
  {
    float sum = 0.f;
#pragma unroll
    for (int t = 0; t < 4; ++t) { sum = dot2(xa[t], 0x3f803f80u, sum); sum = dot2(xb[t], 0x3f803f80u, sum); }
    const float mean = wave_sum(sum) * (1.f / D);
    float qa = 0.f, qb = 0.f;
#pragma unroll
    for (int t = 0; t < 4; ++t) {
      float d0 = lo2f(xa[t]) - mean, d1 = hi2f(xa[t]) - mean, d2 = lo2f(xb[t]) - mean, d3 = hi2f(xb[t]) - mean;
      qa = fmaf(d0, d0, qa); qa = fmaf(d1, d1, qa); qb = fmaf(d2, d2, qb); qb = fmaf(d3, d3, qb);
    }
    const float var = wave_sum(qa + (lane < 16 ? qb : 0.f)) * (1.f / D);
    const float mb = rbf(mean);
    const float sq = rbf(__builtin_sqrtf(rbf(rbf(var) + 1e-5f)));
#pragma unroll
    for (int g = 0; g < 10; ++g) {
      const u32 wd_ = g < 8 ? xn4[g >> 1] : xn1;
      const float xv = (g & 1) ? hi2f(wd_) : lo2f(wd_);
      xdup[g] = dupbf(rbf(xv - mb) / sq);
    }
  }
  TS(2);
  const bool qvalid = ((lane >> 5) & 1) == ((lane >> 3) & 1);
  const int qcol = 8 * c + 4 * ((lane >> 4) & 1), qrow = 2 * w + (lane >> 5);
  {
    f32x4 acc[3] = {{0, 0, 0, 0}, {0, 0, 0, 0}, {0, 0, 0, 0}};
#pragma unroll
    for (int g = 0; g < 10; ++g) {
      const u32x4 a = amask(xdup[g], mask);
#pragma unroll
      for (int i = 0; i < 3; ++i) acc[i] = mfma(a, wqr[g][i], acc[i]);
      SB();
    }
    if (qvalid) {
#pragma unroll
      for (int i = 0; i < 3; ++i) *(f32x4*)&L.qpart[qrow][i * 64 + qcol] = acc[i];
    }
  }
  asm volatile("" :: "s"(tag));
  TS(3);
  barrier();
  if (tid < 192) {
    float t = 0.f;
#pragma unroll
    for (int p = 0; p < 16; ++p) t += L.qpart[p][tid];
    ((unsigned short*)L.qbf)[tid] = (unsigned short)dupbf(t);
  }
  barrier();
  u32x4 q[3];
#pragma unroll
  for (int i = 0; i < 3; ++i) q[i] = *(const u32x4*)&L.qbf[i * 32 + c * 4];
  TS(4);

  const rsrc_t rg = mk(wg + h * 96, D * F * 2 - h * 96);
  const rsrc_t ru = mk(wu + h * 96, D * F * 2 - h * 96);
  const rsrc_t ro = mk(wo + (size_t)h * (DH * D * 2) + s * 320, DH * D * 2 - s * 320);
  const rsrc_t rd = mk(wd + (size_t)h * (48 * D * 2) + s * 320, 48 * D * 2 - s * 320);
  const int guoff = c < 6 ? r * (10 * F * 2) + c * 16 : 0x7ffffff0;
  const int wooff = r * (D * 2) + c * 16, wooff2 = c < 4 ? wooff + 256 : 0x7ffffff0;
  u32x4 gur[10][2], wor[3][3], wdr[6][3];
  f32x4 gacc = {0, 0, 0, 0}, uacc = {0, 0, 0, 0};
  f32x4 oacc[3] = {{0, 0, 0, 0}, {0, 0, 0, 0}, {0, 0, 0, 0}};
  float sc[16];
  float mw = -INFINITY, lw = 0.f;
  u32 pd[16];
  u32 xo = 0;
#pragma unroll
  for (int j = 0; j < 32; ++j) {
    if (j < 16) {
      float a0 = 0.f, a1 = 0.f, a2 = 0.f;
#pragma unroll
      for (int t = 0; t < 4; ++t) { a0 = dot2(kv[j][0][t], q[0][t], a0); a1 = dot2(kv[j][1][t], q[1][t], a1); a2 = dot2(kv[j][2][t], q[2][t], a2); }
      float a = (a0 + a1) + a2;
      a += dpp<XOR1>(a); a += dpp<XOR2>(a); a += dpp<HMIR>(a);
      a = rbf(rbf(a) * 0.07216878364870322f);
      sc[j] = a; mw = fmaxf(mw, a);
    } else {
      const u32x4 a = amask(pd[j - 16], mask);
#pragma unroll
      for (int i = 0; i < 3; ++i) oacc[i] = mfma(a, kv[j][i], oacc[i]);
    }
    if (j - DEPTH >= GLU_AT && j - DEPTH < GLU_AT + 10) {
      const int g = j - DEPTH - GLU_AT;
      const u32x4 a = amask(xdup[g], mask);
      gacc = mfma(a, gur[g][0], gacc); uacc = mfma(a, gur[g][1], uacc);
      if (g == 9 && qvalid) { *(f32x4*)&L.gupart[qrow][qcol] = gacc; *(f32x4*)&L.gupart[qrow][64 + qcol] = uacc; }
    }
    SB();
    if (j >= GLU_AT && j < GLU_AT + 10) {
      const int g = j - GLU_AT;
      gur[g][0] = __builtin_amdgcn_raw_buffer_load_b128(rg, guoff, (w * 80 + g) * (F * 2), AUX_W);
      gur[g][1] = __builtin_amdgcn_raw_buffer_load_b128(ru, guoff, (w * 80 + g) * (F * 2), AUX_W);
      SB();
    }
    if (j + DEPTH >= R0 && j + DEPTH < 32) issue_kv(j + DEPTH);
    if (j == WD_AT && w == 7) {
#pragma unroll
      for (int g = 0; g < 6; ++g) {
        wdr[g][0] = __builtin_amdgcn_raw_buffer_load_b128(rd, wooff, g * (8 * D * 2), AUX_W);
        wdr[g][1] = __builtin_amdgcn_raw_buffer_load_b128(rd, wooff + 128, g * (8 * D * 2), AUX_W);
        wdr[g][2] = __builtin_amdgcn_raw_buffer_load_b128(rd, wooff2, g * (8 * D * 2), AUX_W);
      }
      SB();
    }
    if (j == WO_AT) {
#pragma unroll
      for (int g = 0; g < 3; ++g) {
        wor[g][0] = __builtin_amdgcn_raw_buffer_load_b128(ro, wooff, (w * 3 + g) * (8 * D * 2), AUX_W);
        wor[g][1] = __builtin_amdgcn_raw_buffer_load_b128(ro, wooff + 128, (w * 3 + g) * (8 * D * 2), AUX_W);
        wor[g][2] = __builtin_amdgcn_raw_buffer_load_b128(ro, wooff2, (w * 3 + g) * (8 * D * 2), AUX_W);
      }
      SB();
    }
    if (j == 15) {
      TS(5);
      mw = fmaxf(mw, dpp<ROR8>(mw)); mw = x16_32_max(mw);
      const float mneg = -mw * 1.4426950408889634f;
#pragma unroll
      for (int g = 0; g < 16; ++g) { const float p = __builtin_amdgcn_exp2f(fmaf(sc[g], 1.4426950408889634f, mneg)); lw += p; pd[g] = dupbf(p); }
      lw += dpp<ROR8>(lw); lw = x16_32_sum(lw);
      SB();
    }
  }
  TS(6);
  if (qvalid) {
#pragma unroll
    for (int i = 0; i < 3; ++i) *(f32x4*)&L.opart[qrow][i * 64 + qcol] = oacc[i];
  }
  if (lane == 0) { L.ml[w][0] = mw; L.ml[w][1] = lw; }
  barrier();
  TS(7);
  if (w < 3) {
    float m_[8], l_[8], op[16];
#pragma unroll
    for (int i = 0; i < 8; ++i) { m_[i] = L.ml[i][0]; l_[i] = L.ml[i][1]; }
#pragma unroll
    for (int i = 0; i < 16; ++i) op[i] = L.opart[i][tid];
    SB();
    float M = -INFINITY;
#pragma unroll
    for (int i = 0; i < 8; ++i) M = fmaxf(M, m_[i]);
    float o = 0.f, l = 0.f;
#pragma unroll
    for (int i = 0; i < 8; ++i) {
      const float e = __builtin_amdgcn_exp2f((m_[i] - M) * 1.4426950408889634f);
      o = fmaf(e, op[2 * i] + op[2 * i + 1], o); l = fmaf(e, l_[i], l);
    }
    st_gran(ws, WS_G1 + wgid * 2048 + tid * 8, o, tag);
    if (tid < 2) st_gran(ws, WS_G1 + wgid * 2048 + (192 + tid) * 8, tid ? l : M, tag);
    TS(8);
    u64 po[3], pm[3], pl[3];
    for (u32 spin = 0; spin < SPIN_CAP; ++spin) {
      bool ok = true;
#pragma unroll
      for (int i = 0; i < 3; ++i) {
        const int og = (wgid & ~3) | ((s + 1 + i) & 3);
        po[i] = ld_gran(ws, WS_G1 + og * 2048 + tid * 8);
        pm[i] = ld_gran(ws, WS_G1 + og * 2048 + 192 * 8);
        pl[i] = ld_gran(ws, WS_G1 + og * 2048 + 193 * 8);
      }
#pragma unroll
      for (int i = 0; i < 3; ++i) ok &= (gtag(po[i]) == tag) & (gtag(pm[i]) == tag) & (gtag(pl[i]) == tag);
      if (__all(ok)) break;
    }
    TS(9);
    const float MM = fmaxf(fmaxf(M, gval(pm[0])), fmaxf(gval(pm[1]), gval(pm[2])));
    const float e0 = __builtin_amdgcn_exp2f((M - MM) * 1.4426950408889634f);
    float O = e0 * o, Lsum = e0 * l;
#pragma unroll
    for (int k = 0; k < 3; ++k) { const float e = __builtin_amdgcn_exp2f((gval(pm[k]) - MM) * 1.4426950408889634f); O = fmaf(e, gval(po[k]), O); Lsum = fmaf(e, gval(pl[k]), Lsum); }
    L.obf[tid] = dupbf(O / Lsum);
  } else if (w == 7) {
    float g = 0.f, u = 0.f;
#pragma unroll
    for (int p = 0; p < 16; ++p) { g += L.gupart[p][lane]; u += L.gupart[p][64 + lane]; }
    L.hbf[lane] = dupbf(rbf(g) * rbf(u));
    asm volatile("s_waitcnt lgkmcnt(0)" ::: "memory");
    f32x4 y[3] = {{0, 0, 0, 0}, {0, 0, 0, 0}, {0, 0, 0, 0}};
#pragma unroll
    for (int gg = 0; gg < 6; ++gg) {
      const u32x4 a = amask(L.hbf[gg * 8 + r], mask);
#pragma unroll
      for (int i = 0; i < 3; ++i) y[i] = mfma(a, wdr[gg][i], y[i]);
    }
    const bool b3 = lane & 8, b5 = lane & 32;
#pragma unroll
    for (int i = 0; i < 3; ++i) {
      float f[4];
#pragma unroll
      for (int t = 0; t < 4; ++t) {
        const float v = qvalid ? y[i][t] : 0.f;
        const float v2 = v + dpp<ROR8>(v);
        auto sw = __builtin_amdgcn_permlane32_swap(__float_as_uint(v2), __float_as_uint(v2), false, false);
        f[t] = __uint_as_float(sw[0]) + __uint_as_float(sw[1]);
      }
      const float lo = b3 ? f[1] : f[0], hi = b3 ? f[3] : f[2];
      const float v = b5 ? hi : lo;
      const int col = i * 64 + qcol + (b5 ? 2 : 0) + (b3 ? 1 : 0);
      if (col < 160) st_gran(ws, WS_G2Y + ((b * H + h) * D + s * 160 + col) * 8, v, tag);
    }
  }
  barrier();
  TS(10);
  {
    f32x4 sa[3] = {{0, 0, 0, 0}, {0, 0, 0, 0}, {0, 0, 0, 0}};
    u32 ov[3];
#pragma unroll
    for (int g = 0; g < 3; ++g) ov[g] = L.obf[(w * 3 + g) * 8 + r];
#pragma unroll
    for (int g = 0; g < 3; ++g) {
      const u32x4 a = amask(ov[g], mask);
#pragma unroll
      for (int i = 0; i < 3; ++i) sa[i] = mfma(a, wor[g][i], sa[i]);
    }
    if (qvalid) {
#pragma unroll
      for (int i = 0; i < 3; ++i) *(f32x4*)&L.spart[qrow][i * 64 + qcol] = sa[i];
    }
  }
  barrier();
  TS(11);
  if (tid < 160) {
    float sp[16];
#pragma unroll
    for (int p = 0; p < 16; ++p) sp[p] = L.spart[p][tid];
    SB();
#pragma unroll
    for (int p = 0; p < 8; ++p) sp[p] += sp[p + 8];
#pragma unroll
    for (int p = 0; p < 4; ++p) sp[p] += sp[p + 4];
    st_gran(ws, WS_G2S + ((b * H + h) * D + s * 160 + tid) * 8, (sp[0] + sp[1]) + (sp[2] + sp[3]), tag);
  }
  TS(12);
  const int idx = h * 4 + s;
  if (w == 3 && (idx & 7) < 5) {
    const int task = (idx >> 3) * 5 + (idx & 7);
    const u32 xv = __builtin_amdgcn_raw_buffer_load_b16(rx, (task * 8 + c) * 2, 0, 0);
    const u32 voff = ((b * H + r) * D + task * 8 + c) * 8;
    u64 ps[4], py[4];
    for (u32 spin = 0; spin < SPIN_CAP; ++spin) {
      bool ok = true;
#pragma unroll
      for (int i = 0; i < 4; ++i) {
        ps[i] = ld_gran(ws, WS_G2S + voff + i * 8 * D * 8);
        py[i] = ld_gran(ws, WS_G2Y + voff + i * 8 * D * 8);
      }
#pragma unroll
      for (int i = 0; i < 4; ++i) ok &= (gtag(ps[i]) == tag) & (gtag(py[i]) == tag);
      if (__all(ok)) break;
    }
    TS(13);
    float sv = (gval(ps[0]) + gval(ps[1])) + (gval(ps[2]) + gval(ps[3]));
    float yv = (gval(py[0]) + gval(py[1])) + (gval(py[2]) + gval(py[3]));
    sv += dpp<ROR8>(sv); yv += dpp<ROR8>(yv);
    sv = rbf(x16_32_sum(sv)); yv = rbf(x16_32_sum(yv));
    const float gate = rbf(yv / (1.f + __builtin_amdgcn_exp2f(-yv * 1.4426950408889634f)));
    const float res = lo2f(xv) + rbf(sv * gate);
    if (lane < 8) ((unsigned short*)out)[b * D + task * 8 + c] = (unsigned short)dupbf(res);
    if (task == 0 && lane == 0) __hip_atomic_store((gu32*)(ws + WS_EPOCH + b * 256), tag, __ATOMIC_RELAXED, __HIP_MEMORY_SCOPE_AGENT);
  }
}
}

extern "C" void kernel_launch(void* const* d_in, const int* in_sizes, int n_in, void* d_out, int out_size, void* d_ws, size_t ws_size, hipStream_t stream) {
  fused_decode<<<256, NW * 64, 0, stream>>>((const char*)d_in[0], (const char*)d_in[1], (const char*)d_in[2], (const char*)d_in[3], (const char*)d_in[4],
                                            (const char*)d_in[6], (const char*)d_in[7], (const char*)d_in[8], (char*)d_out, (char*)d_ws);
}
```

```cpp
#include <hip/hip_runtime.h>
#include <hip/hip_bf16.h>
#include <stdint.h>

#define AUX_KV 2
#define AUX_W 0
#define R0 5
#define DEPTH 3
#define KEARLY 3
#define GLU_AT 2
#define WD_AT 17
#define WO_AT 29

namespace {
constexpr int D = 640, H = 32, SK = 4096, DH = 192, F = 1536;
constexpr int NW = 8;
typedef unsigned u32;
typedef unsigned long long u64;
typedef u32 u32x2 __attribute__((ext_vector_type(2)));
typedef u32 u32x4 __attribute__((ext_vector_type(4)));
typedef float f32x4 __attribute__((ext_vector_type(4)));
typedef __bf16 bf16x2 __attribute__((ext_vector_type(2)));
typedef __bf16 bf16x8 __attribute__((ext_vector_type(8)));
typedef __amdgpu_buffer_rsrc_t rsrc_t;
#define SB() __builtin_amdgcn_sched_barrier(0)

constexpr u32 WS_EPOCH = 0;
constexpr u32 WS_G1 = 4096;
constexpr u32 WS_G2S = WS_G1 + 256 * 256 * 8;
constexpr u32 WS_G2Y = WS_G2S + 2 * 32 * 640 * 8;
constexpr u32 WS_END = WS_G2Y + 2 * 32 * 640 * 8;
constexpr u32 WS_STAMP = 2u << 20;
constexpr u32 SPIN_CAP = 1u << 20;

__device__ __forceinline__ rsrc_t mk(const void* p, u32 bytes) { return __builtin_amdgcn_make_buffer_rsrc(const_cast<void*>(p), 0, (int)bytes, 0x00020000); }
__device__ __forceinline__ float dot2(u32 a, u32 b, float c) { return __builtin_amdgcn_fdot2_f32_bf16(__builtin_bit_cast(bf16x2, a), __builtin_bit_cast(bf16x2, b), c, false); }
__device__ __forceinline__ f32x4 mfma(u32x4 a, u32x4 b, f32x4 c) { return __builtin_amdgcn_mfma_f32_16x16x32_bf16(__builtin_bit_cast(bf16x8, a), __builtin_bit_cast(bf16x8, b), c, 0, 0, 0); }
template <int C> __device__ __forceinline__ float dpp(float x) { return __builtin_bit_cast(float, __builtin_amdgcn_mov_dpp(__builtin_bit_cast(int, x), C, 0xf, 0xf, true)); }
constexpr int XOR1 = 0xB1, XOR2 = 0x4E, HMIR = 0x141, ROR8 = 0x128;
__device__ __forceinline__ float lo2f(u32 v) { return __uint_as_float(v << 16); }
__device__ __forceinline__ float hi2f(u32 v) { return __uint_as_float(v & 0xffff0000u); }
__device__ __forceinline__ float rbf(float f) { return (float)(__bf16)f; }
__device__ __forceinline__ u32 dupbf(float f) { bf16x2 v = {(__bf16)f, (__bf16)f}; return __builtin_bit_cast(u32, v); }
__device__ __forceinline__ float x16_32_sum(float x) {
  auto s = __builtin_amdgcn_permlane16_swap(__float_as_uint(x), __float_as_uint(x), false, false);
  x = __uint_as_float(s[0]) + __uint_as_float(s[1]);
  auto t = __builtin_amdgcn_permlane32_swap(__float_as_uint(x), __float_as_uint(x), false, false);
  return __uint_as_float(t[0]) + __uint_as_float(t[1]);
}
__device__ __forceinline__ float x16_32_max(float x) {
  auto s = __builtin_amdgcn_permlane16_swap(__float_as_uint(x), __float_as_uint(x), false, false);
  x = fmaxf(__uint_as_float(s[0]), __uint_as_float(s[1]));
  auto t = __builtin_amdgcn_permlane32_swap(__float_as_uint(x), __float_as_uint(x), false, false);
  return fmaxf(__uint_as_float(t[0]), __uint_as_float(t[1]));
}
__device__ __forceinline__ float wave_sum(float x) { x += dpp<XOR1>(x); x += dpp<XOR2>(x); x += dpp<HMIR>(x); x += dpp<ROR8>(x); return x16_32_sum(x); }
__device__ __forceinline__ void barrier() { asm volatile("s_waitcnt lgkmcnt(0)\n\ts_barrier" ::: "memory"); }
__device__ __forceinline__ u32x4 amask(u32 dup, const u32 (&m)[4]) { return u32x4{dup & m[0], dup & m[1], dup & m[2], dup & m[3]}; }

typedef __attribute__((address_space(1))) u64 gu64;
typedef __attribute__((address_space(1))) u32 gu32;
__device__ __forceinline__ void st_gran(char* ws, u32 off, float v, u32 tag) {
  __hip_atomic_store((gu64*)(ws + off), ((u64)tag << 32) | __float_as_uint(v), __ATOMIC_RELAXED, __HIP_MEMORY_SCOPE_AGENT);
}
__device__ __forceinline__ u64 ld_gran(char* ws, u32 off) { return __hip_atomic_load((gu64*)(ws + off), __ATOMIC_RELAXED, __HIP_MEMORY_SCOPE_AGENT); }
__device__ __forceinline__ float gval(u64 g) { return __uint_as_float((u32)g); }
__device__ __forceinline__ u32 gtag(u64 g) { return (u32)(g >> 32); }

struct Lds {
  float qpart[16][192];
  float opart[16][192];
  float spart[16][192];
  float gupart[16][128];
  float ml[8][2];
  u32 qbf[96];
  u32 obf[192];
  u32 hbf[64];
};

#define LEAK 1
#define TS(i) do { SB(); u64 t_ = __builtin_amdgcn_s_memrealtime(); asm volatile("" : "+s"(t_)); ts[i] = (u32)t_; SB(); } while (0)

__global__ void __launch_bounds__(NW * 64, 2) fused_decode(const char* __restrict__ x, const char* __restrict__ K, const char* __restrict__ V,
    const char* __restrict__ wq, const char* __restrict__ wo, const char* __restrict__ wg, const char* __restrict__ wu,
    const char* __restrict__ wd, char* __restrict__ out, char* ws) {
  __shared__ __attribute__((aligned(16))) Lds L;
  u32 ts[16];
#pragma unroll
  for (int i = 0; i < 16; ++i) ts[i] = 0;
  TS(0);
  asm volatile("" :: "s"(x), "s"(K), "s"(V), "s"(wq), "s"(wo), "s"(wg), "s"(wu), "s"(wd), "s"(out), "s"(ws));
  const int tid = threadIdx.x, lane = tid & 63, w = __builtin_amdgcn_readfirstlane(tid >> 6);
  const int r = lane >> 3, c = lane & 7;
  const int xl = blockIdx.x & 7, rk = blockIdx.x >> 3;
  const int h = xl * 4 + (rk >> 3), b = (rk >> 2) & 1, s = rk & 3;
  const int wgid = (b * H + h) * 4 + s;

  const rsrc_t rx = mk(x + b * (D * 2), D * 2);
  const rsrc_t rq = mk(wq + h * (DH * 2), D * H * DH * 2 - h * (DH * 2));
  const rsrc_t rK = mk(K + (size_t)wgid * (1024 * DH * 2), 1024 * DH * 2);
  const rsrc_t rV = mk(V + (size_t)wgid * (1024 * DH * 2), 1024 * DH * 2);

  const u32x4 xa = __builtin_amdgcn_raw_buffer_load_b128(rx, lane * 16, 0, 0);
  const u32x4 xb = __builtin_amdgcn_raw_buffer_load_b128(rx, lane * 16 + 1024, 0, 0);
  const u32x4 xn4 = __builtin_amdgcn_raw_buffer_load_b128(rx, r * 20, w * 160, 0);
  const u32 xn1 = __builtin_amdgcn_raw_buffer_load_b32(rx, r * 20 + 16, w * 160, 0);
  const u32 epoch = __hip_atomic_load((gu32*)(ws + WS_EPOCH + b * 256), __ATOMIC_RELAXED, __HIP_MEMORY_SCOPE_AGENT);
  SB();
  const int kvoff = r * 384 + c * 16;
  u32x4 kv[32][3];
  auto issue_kv = [&](int j) {
#pragma unroll
    for (int i = 0; i < 3; ++i) kv[j][i] = __builtin_amdgcn_raw_buffer_load_b128(j < 16 ? rK : rV, kvoff + i * 128, ((j & 15) * NW + w) * 3072, AUX_KV);
    SB();
  };
  u32x4 wqr[10][3];
  const int wqoff = r * (10 * H * DH * 2) + c * 16;
#pragma unroll
  for (int g = 0; g < 10; ++g) {
    if (g == KEARLY) issue_kv(0);
#pragma unroll
    for (int i = 0; i < 3; ++i) wqr[g][i] = __builtin_amdgcn_raw_buffer_load_b128(rq, wqoff + i * 128, (w * 80 + g) * (H * DH * 2), AUX_W);
    SB();
  }
#pragma unroll
  for (int j = (KEARLY < 10 ? 1 : 0); j < R0; ++j) issue_kv(j);
  TS(1);
  const u32 tag = __builtin_amdgcn_readfirstlane(epoch) + 0x9E3779B9u;

  u32 mask[4];
#pragma unroll
  for (int t = 0; t < 4; ++t) mask[t] = ((c >> 1) == t) ? ((c & 1) ? 0xffff0000u : 0x0000ffffu) : 0u;
  u32 xdup[10];
  {
    float sum = 0.f;
#pragma unroll
    for (int t = 0; t < 4; ++t) { sum = dot2(xa[t], 0x3f803f80u, sum); sum = dot2(xb[t], 0x3f803f80u, sum); }
    const float mean = wave_sum(sum) * (1.f / D);
    float qa = 0.f, qb = 0.f;
#pragma unroll
    for (int t = 0; t < 4; ++t) {
      float d0 = lo2f(xa[t]) - mean, d1 = hi2f(xa[t]) - mean, d2 = lo2f(xb[t]) - mean, d3 = hi2f(xb[t]) - mean;
      qa = fmaf(d0, d0, qa); qa = fmaf(d1, d1, qa); qb = fmaf(d2, d2, qb); qb = fmaf(d3, d3, qb);
    }
    const float var = wave_sum(qa + (lane < 16 ? qb : 0.f)) * (1.f / D);
    const float mb = rbf(mean);
    const float sq = rbf(__builtin_sqrtf(rbf(rbf(var) + 1e-5f)));
#pragma unroll
    for (int g = 0; g < 10; ++g) {
      const u32 wd_ = g < 8 ? xn4[g >> 1] : xn1;
      const float xv = (g & 1) ? hi2f(wd_) : lo2f(wd_);
      xdup[g] = dupbf(rbf(xv - mb) / sq);
    }
  }
  TS(2);
  const bool qvalid = ((lane >> 5) & 1) == ((lane >> 3) & 1);
  const int qcol = 8 * c + 4 * ((lane >> 4) & 1), qrow = 2 * w + (lane >> 5);
  {
    f32x4 acc[3] = {{0, 0, 0, 0}, {0, 0, 0, 0}, {0, 0, 0, 0}};
#pragma unroll
    for (int g = 0; g < 10; ++g) {
      const u32x4 a = amask(xdup[g], mask);
#pragma unroll
      for (int i = 0; i < 3; ++i) acc[i] = mfma(a, wqr[g][i], acc[i]);
      SB();
    }
    if (qvalid) {
#pragma unroll
      for (int i = 0; i < 3; ++i) *(f32x4*)&L.qpart[qrow][i * 64 + qcol] = acc[i];
    }
  }
  asm volatile("" :: "s"(tag));
  TS(3);
  barrier();
  if (tid < 192) {
    float t = 0.f;
#pragma unroll
    for (int p = 0; p < 16; ++p) t += L.qpart[p][tid];
    ((unsigned short*)L.qbf)[tid] = (unsigned short)dupbf(t);
  }
  barrier();
  u32x4 q[3];
#pragma unroll
  for (int i = 0; i < 3; ++i) q[i] = *(const u32x4*)&L.qbf[i * 32 + c * 4];
  TS(4);

  const rsrc_t rg = mk(wg + h * 96, D * F * 2 - h * 96);
  const rsrc_t ru = mk(wu + h * 96, D * F * 2 - h * 96);
  const rsrc_t ro = mk(wo + (size_t)h * (DH * D * 2) + s * 320, DH * D * 2 - s * 320);
  const rsrc_t rd = mk(wd + (size_t)h * (48 * D * 2) + s * 320, 48 * D * 2 - s * 320);
  const int guoff = c < 6 ? r * (10 * F * 2) + c * 16 : 0x7ffffff0;
  const int wooff = r * (D * 2) + c * 16, wooff2 = c < 4 ? wooff + 256 : 0x7ffffff0;
  u32x4 gur[10][2], wor[3][3], wdr[6][3];
  f32x4 gacc = {0, 0, 0, 0}, uacc = {0, 0, 0, 0};
  f32x4 oacc[3] = {{0, 0, 0, 0}, {0, 0, 0, 0}, {0, 0, 0, 0}};
  float sc[16];
  float mw = -INFINITY, lw = 0.f;
  u32 pd[16];
  u32 xo = 0;
#pragma unroll
  for (int j = 0; j < 32; ++j) {
    if (j < 16) {
      float a0 = 0.f, a1 = 0.f, a2 = 0.f;
#pragma unroll
      for (int t = 0; t < 4; ++t) { a0 = dot2(kv[j][0][t], q[0][t], a0); a1 = dot2(kv[j][1][t], q[1][t], a1); a2 = dot2(kv[j][2][t], q[2][t], a2); }
      float a = (a0 + a1) + a2;
      a += dpp<XOR1>(a); a += dpp<XOR2>(a); a += dpp<HMIR>(a);
      a = rbf(rbf(a) * 0.07216878364870322f);
      sc[j] = a; mw = fmaxf(mw, a);
    } else {
      const u32x4 a = amask(pd[j - 16], mask);
#pragma unroll
      for (int i = 0; i < 3; ++i) oacc[i] = mfma(a, kv[j][i], oacc[i]);
    }
    if (j - DEPTH >= GLU_AT && j - DEPTH < GLU_AT + 10) {
      const int g = j - DEPTH - GLU_AT;
      const u32x4 a = amask(xdup[g], mask);
      gacc = mfma(a, gur[g][0], gacc); uacc = mfma(a, gur[g][1], uacc);
      if (g == 9 && qvalid) { *(f32x4*)&L.gupart[qrow][qcol] = gacc; *(f32x4*)&L.gupart[qrow][64 + qcol] = uacc; }
    }
    SB();
    if (j >= GLU_AT && j < GLU_AT + 10) {
      const int g = j - GLU_AT;
      gur[g][0] = __builtin_amdgcn_raw_buffer_load_b128(rg, guoff, (w * 80 + g) * (F * 2), AUX_W);
      gur[g][1] = __builtin_amdgcn_raw_buffer_load_b128(ru, guoff, (w * 80 + g) * (F * 2), AUX_W);
      SB();
    }
    if (j + DEPTH >= R0 && j + DEPTH < 32) issue_kv(j + DEPTH);
    if (j == WD_AT && w == 7) {
#pragma unroll
      for (int g = 0; g < 6; ++g) {
        wdr[g][0] = __builtin_amdgcn_raw_buffer_load_b128(rd, wooff, g * (8 * D * 2), AUX_W);
        wdr[g][1] = __builtin_amdgcn_raw_buffer_load_b128(rd, wooff + 128, g * (8 * D * 2), AUX_W);
        wdr[g][2] = __builtin_amdgcn_raw_buffer_load_b128(rd, wooff2, g * (8 * D * 2), AUX_W);
      }
      SB();
    }
    if (j == WO_AT) {
#pragma unroll
      for (int g = 0; g < 3; ++g) {
        wor[g][0] = __builtin_amdgcn_raw_buffer_load_b128(ro, wooff, (w * 3 + g) * (8 * D * 2), AUX_W);
        wor[g][1] = __builtin_amdgcn_raw_buffer_load_b128(ro, wooff + 128, (w * 3 + g) * (8 * D * 2), AUX_W);
        wor[g][2] = __builtin_amdgcn_raw_buffer_load_b128(ro, wooff2, (w * 3 + g) * (8 * D * 2), AUX_W);
      }
      SB();
    }
    if (j == 15) {
      TS(5);
      mw = fmaxf(mw, dpp<ROR8>(mw)); mw = x16_32_max(mw);
      const float mneg = -mw * 1.4426950408889634f;
#pragma unroll
      for (int g = 0; g < 16; ++g) { const float p = __builtin_amdgcn_exp2f(fmaf(sc[g], 1.4426950408889634f, mneg)); lw += p; pd[g] = dupbf(p); }
      lw += dpp<ROR8>(lw); lw = x16_32_sum(lw);
      SB();
    }
  }
  TS(6);
  if (qvalid) {
#pragma unroll
    for (int i = 0; i < 3; ++i) *(f32x4*)&L.opart[qrow][i * 64 + qcol] = oacc[i];
  }
  if (lane == 0) { L.ml[w][0] = mw; L.ml[w][1] = lw; }
  barrier();
  TS(7);
  if (w < 3) {
    float m_[8], l_[8], op[16];
#pragma unroll
    for (int i = 0; i < 8; ++i) { m_[i] = L.ml[i][0]; l_[i] = L.ml[i][1]; }
#pragma unroll
    for (int i = 0; i < 16; ++i) op[i] = L.opart[i][tid];
    SB();
    float M = -INFINITY;
#pragma unroll
    for (int i = 0; i < 8; ++i) M = fmaxf(M, m_[i]);
    float o = 0.f, l = 0.f;
#pragma unroll
    for (int i = 0; i < 8; ++i) {
      const float e = __builtin_amdgcn_exp2f((m_[i] - M) * 1.4426950408889634f);
      o = fmaf(e, op[2 * i] + op[2 * i + 1], o); l = fmaf(e, l_[i], l);
    }
    st_gran(ws, WS_G1 + wgid * 2048 + tid * 8, o, tag);
    if (tid < 2) st_gran(ws, WS_G1 + wgid * 2048 + (192 + tid) * 8, tid ? l : M, tag);
    TS(8);
    u64 po[3], pm[3], pl[3];
    for (u32 spin = 0; spin < SPIN_CAP; ++spin) {
      bool ok = true;
#pragma unroll
      for (int i = 0; i < 3; ++i) {
        const int og = (wgid & ~3) | ((s + 1 + i) & 3);
        po[i] = ld_gran(ws, WS_G1 + og * 2048 + tid * 8);
        pm[i] = ld_gran(ws, WS_G1 + og * 2048 + 192 * 8);
        pl[i] = ld_gran(ws, WS_G1 + og * 2048 + 193 * 8);
      }
#pragma unroll
      for (int i = 0; i < 3; ++i) ok &= (gtag(po[i]) == tag) & (gtag(pm[i]) == tag) & (gtag(pl[i]) == tag);
      if (__all(ok)) break;
    }
    TS(9);
    const float MM = fmaxf(fmaxf(M, gval(pm[0])), fmaxf(gval(pm[1]), gval(pm[2])));
    const float e0 = __builtin_amdgcn_exp2f((M - MM) * 1.4426950408889634f);
    float O = e0 * o, Lsum = e0 * l;
#pragma unroll
    for (int k = 0; k < 3; ++k) { const float e = __builtin_amdgcn_exp2f((gval(pm[k]) - MM) * 1.4426950408889634f); O = fmaf(e, gval(po[k]), O); Lsum = fmaf(e, gval(pl[k]), Lsum); }
    L.obf[tid] = dupbf(O / Lsum);
  } else if (w == 7) {
    float g = 0.f, u = 0.f;
#pragma unroll
    for (int p = 0; p < 16; ++p) { g += L.gupart[p][lane]; u += L.gupart[p][64 + lane]; }
    L.hbf[lane] = dupbf(rbf(g) * rbf(u));
    asm volatile("s_waitcnt lgkmcnt(0)" ::: "memory");
    f32x4 y[3] = {{0, 0, 0, 0}, {0, 0, 0, 0}, {0, 0, 0, 0}};
#pragma unroll
    for (int gg = 0; gg < 6; ++gg) {
      const u32x4 a = amask(L.hbf[gg * 8 + r], mask);
#pragma unroll
      for (int i = 0; i < 3; ++i) y[i] = mfma(a, wdr[gg][i], y[i]);
    }
    const bool b3 = lane & 8, b5 = lane & 32;
#pragma unroll
    for (int i = 0; i < 3; ++i) {
      float f[4];
#pragma unroll
      for (int t = 0; t < 4; ++t) {
        const float v = qvalid ? y[i][t] : 0.f;
        const float v2 = v + dpp<ROR8>(v);
        auto sw = __builtin_amdgcn_permlane32_swap(__float_as_uint(v2), __float_as_uint(v2), false, false);
        f[t] = __uint_as_float(sw[0]) + __uint_as_float(sw[1]);
      }
      const float lo = b3 ? f[1] : f[0], hi = b3 ? f[3] : f[2];
      const float v = b5 ? hi : lo;
      const int col = i * 64 + qcol + (b5 ? 2 : 0) + (b3 ? 1 : 0);
      if (col < 160) st_gran(ws, WS_G2Y + ((b * H + h) * D + s * 160 + col) * 8, v, tag);
    }
  }
  barrier();
  TS(10);
  {
    f32x4 sa[3] = {{0, 0, 0, 0}, {0, 0, 0, 0}, {0, 0, 0, 0}};
    u32 ov[3];
#pragma unroll
    for (int g = 0; g < 3; ++g) ov[g] = L.obf[(w * 3 + g) * 8 + r];
#pragma unroll
    for (int g = 0; g < 3; ++g) {
      const u32x4 a = amask(ov[g], mask);
#pragma unroll
      for (int i = 0; i < 3; ++i) sa[i] = mfma(a, wor[g][i], sa[i]);
    }
    if (qvalid) {
#pragma unroll
      for (int i = 0; i < 3; ++i) *(f32x4*)&L.spart[qrow][i * 64 + qcol] = sa[i];
    }
  }
  barrier();
  TS(11);
  if (tid < 160) {
    float sp[16];
#pragma unroll
    for (int p = 0; p < 16; ++p) sp[p] = L.spart[p][tid];
    SB();
#pragma unroll
    for (int p = 0; p < 8; ++p) sp[p] += sp[p + 8];
#pragma unroll
    for (int p = 0; p < 4; ++p) sp[p] += sp[p + 4];
    st_gran(ws, WS_G2S + ((b * H + h) * D + s * 160 + tid) * 8, (sp[0] + sp[1]) + (sp[2] + sp[3]), tag);
  }
  TS(12);
  const int idx = h * 4 + s;
  if (w == 3 && (idx & 7) < 5) {
    const int task = (idx >> 3) * 5 + (idx & 7);
    const u32 xv = __builtin_amdgcn_raw_buffer_load_b16(rx, (task * 8 + c) * 2, 0, 0);
    const u32 voff = ((b * H + r) * D + task * 8 + c) * 8;
    u64 ps[4], py[4];
    for (u32 spin = 0; spin < SPIN_CAP; ++spin) {
      bool ok = true;
#pragma unroll
      for (int i = 0; i < 4; ++i) {
        ps[i] = ld_gran(ws, WS_G2S + voff + i * 8 * D * 8);
        py[i] = ld_gran(ws, WS_G2Y + voff + i * 8 * D * 8);
      }
#pragma unroll
      for (int i = 0; i < 4; ++i) ok &= (gtag(ps[i]) == tag) & (gtag(py[i]) == tag);
      if (__all(ok)) break;
    }
    TS(13);
    float sv = (gval(ps[0]) + gval(ps[1])) + (gval(ps[2]) + gval(ps[3]));
    float yv = (gval(py[0]) + gval(py[1])) + (gval(py[2]) + gval(py[3]));
    sv += dpp<ROR8>(sv); yv += dpp<ROR8>(yv);
    sv = rbf(x16_32_sum(sv)); yv = rbf(x16_32_sum(yv));
    const float gate = rbf(yv / (1.f + __builtin_amdgcn_exp2f(-yv * 1.4426950408889634f)));
    const float res = lo2f(xv) + rbf(sv * gate);
    if (lane < 8) ((unsigned short*)out)[b * D + task * 8 + c] = (unsigned short)dupbf(res);
    if (task == 0 && lane == 0) __hip_atomic_store((gu32*)(ws + WS_EPOCH + b * 256), tag, __ATOMIC_RELAXED, __HIP_MEMORY_SCOPE_AGENT);
  }
  TS(14);
  if (blockIdx.x == 0 && w == 3) {
    const u32 d = (ts[0] - ts[0]) * 0;
    while ((u32)__builtin_amdgcn_s_memrealtime() - ts[14] < d) __builtin_amdgcn_s_sleep(1);
  }

}
}

extern "C" void kernel_launch(void* const* d_in, const int* in_sizes, int n_in, void* d_out, int out_size, void* d_ws, size_t ws_size, hipStream_t stream) {
  fused_decode<<<256, NW * 64, 0, stream>>>((const char*)d_in[0], (const char*)d_in[1], (const char*)d_in[2], (const char*)d_in[3], (const char*)d_in[4],
                                            (const char*)d_in[6], (const char*)d_in[7], (const char*)d_in[8], (char*)d_out, (char*)d_ws);
}
```

```cpp
#include <hip/hip_runtime.h>
#include <hip/hip_bf16.h>
#include <stdint.h>
#include <type_traits>

#define AUX_KV 2
#define AUX_WQ 2
#define AUX_WO 0
#define PARK 14
#define RING 8
#define DELAY 45
#define LVL 99
#define PACE 0
#define TSLEEP 0
#define GLU_DELAY 100
#define WO_LATE 0
#define WO_AT (63 - RING)

namespace {
constexpr int D = 640, H = 32, SK = 4096, DH = 192, F = 1536;
constexpr int NW = 8;
constexpr float LOG2E = 1.4426950408889634f;
typedef unsigned u32;
typedef unsigned long long u64;
typedef u32 u32x2 __attribute__((ext_vector_type(2)));
typedef u32 u32x4 __attribute__((ext_vector_type(4)));
typedef float f32x4 __attribute__((ext_vector_type(4)));
typedef __bf16 bf16x2 __attribute__((ext_vector_type(2)));
typedef __bf16 bf16x8 __attribute__((ext_vector_type(8)));
typedef __amdgpu_buffer_rsrc_t rsrc_t;
typedef __attribute__((address_space(1))) u64 gu64;
typedef __attribute__((address_space(1))) u32 gu32;
#define SB() __builtin_amdgcn_sched_barrier(0)

constexpr u32 WS_EPOCH = 0;
constexpr u32 WS_Q = 4096;
constexpr u32 WS_GU = WS_Q + 32 * 2 * 8 * 192 * 8;
constexpr u32 WS_H = WS_GU + 32 * 8 * 192 * 8;
constexpr u32 WS_ML = WS_H + 32 * 64 * 8;
constexpr u32 WS_G1 = WS_ML + 256 * 16 * 8;
constexpr u32 WS_G2S = WS_G1 + 256 * 192 * 8;
constexpr u32 WS_G2Y = WS_G2S + 2 * 32 * 640 * 8;
constexpr u32 WS_END = WS_G2Y + 2 * 32 * 640 * 8;
constexpr u32 WS_STAMP = 4u << 20;
constexpr u32 SPIN_CAP = 1u << 20;
constexpr int OOB = 0x7ffffff0;

__device__ __forceinline__ rsrc_t mk(const void* p, u32 bytes) { return __builtin_amdgcn_make_buffer_rsrc(const_cast<void*>(p), 0, (int)bytes, 0x00020000); }
__device__ __forceinline__ float dot2(u32 a, u32 b, float c) { return __builtin_amdgcn_fdot2_f32_bf16(__builtin_bit_cast(bf16x2, a), __builtin_bit_cast(bf16x2, b), c, false); }
__device__ __forceinline__ f32x4 mfma(u32x4 a, u32x4 b, f32x4 c) { return __builtin_amdgcn_mfma_f32_16x16x32_bf16(__builtin_bit_cast(bf16x8, a), __builtin_bit_cast(bf16x8, b), c, 0, 0, 0); }
template <int C> __device__ __forceinline__ float dpp(float x) { return __builtin_bit_cast(float, __builtin_amdgcn_mov_dpp(__builtin_bit_cast(int, x), C, 0xf, 0xf, true)); }
constexpr int XOR1 = 0xB1, XOR2 = 0x4E, HMIR = 0x141, ROR8 = 0x128;
__device__ __forceinline__ float lo2f(u32 v) { return __uint_as_float(v << 16); }
__device__ __forceinline__ float hi2f(u32 v) { return __uint_as_float(v & 0xffff0000u); }
__device__ __forceinline__ float rbf(float f) { return (float)(__bf16)f; }
__device__ __forceinline__ u32 dupbf(float f) { bf16x2 v = {(__bf16)f, (__bf16)f}; return __builtin_bit_cast(u32, v); }
__device__ __forceinline__ float x16_sum(float x) {
  auto s = __builtin_amdgcn_permlane16_swap(__float_as_uint(x), __float_as_uint(x), false, false);
  return __uint_as_float(s[0]) + __uint_as_float(s[1]);
}
__device__ __forceinline__ float x32_sum(float x) {
  auto t = __builtin_amdgcn_permlane32_swap(__float_as_uint(x), __float_as_uint(x), false, false);
  return __uint_as_float(t[0]) + __uint_as_float(t[1]);
}
__device__ __forceinline__ float x16_32_max(float x) {
  auto s = __builtin_amdgcn_permlane16_swap(__float_as_uint(x), __float_as_uint(x), false, false);
  x = fmaxf(__uint_as_float(s[0]), __uint_as_float(s[1]));
  auto t = __builtin_amdgcn_permlane32_swap(__float_as_uint(x), __float_as_uint(x), false, false);
  return fmaxf(__uint_as_float(t[0]), __uint_as_float(t[1]));
}
__device__ __forceinline__ float half_sum(float x) { x += dpp<XOR1>(x); x += dpp<XOR2>(x); x += dpp<HMIR>(x); x += dpp<ROR8>(x); return x16_sum(x); }
__device__ __forceinline__ void barrier() { asm volatile("s_waitcnt lgkmcnt(0)\n\ts_barrier" ::: "memory"); }
__device__ __forceinline__ u32x4 amask(u32 dup, const u32 (&m)[4]) { return u32x4{dup & m[0], dup & m[1], dup & m[2], dup & m[3]}; }
__device__ __forceinline__ void fold2(f32x4 a, f32x4 b, bool b3, float& e, float& o) {
  auto p = __builtin_amdgcn_permlane32_swap(__float_as_uint(a[0]), __float_as_uint(a[2]), false, false);
  auto q = __builtin_amdgcn_permlane32_swap(__float_as_uint(a[1]), __float_as_uint(a[3]), false, false);
  auto u = __builtin_amdgcn_permlane32_swap(__float_as_uint(b[0]), __float_as_uint(b[2]), false, false);
  auto v = __builtin_amdgcn_permlane32_swap(__float_as_uint(b[1]), __float_as_uint(b[3]), false, false);
  const float e0 = __uint_as_float(p[0]) + dpp<ROR8>(__uint_as_float(p[1])), o0 = __uint_as_float(q[0]) + dpp<ROR8>(__uint_as_float(q[1]));
  const float e1 = __uint_as_float(u[1]) + dpp<ROR8>(__uint_as_float(u[0])), o1 = __uint_as_float(v[1]) + dpp<ROR8>(__uint_as_float(v[0]));
  e = b3 ? e1 : e0; o = b3 ? o1 : o0;
}
__device__ __forceinline__ void st_gran2(rsrc_t rw, u32 off, float v0, float v1, u32 tag) {
  __builtin_amdgcn_raw_buffer_store_b128(u32x4{__float_as_uint(v0), tag, __float_as_uint(v1), tag}, rw, off, 0, 16);
}
__device__ __forceinline__ void ld_gran2(u32x4 (&g)[1], const char* ws, const u32 (&o)[1]) {
  asm volatile("global_load_dwordx4 %0, %1, %2 sc1\n\ts_waitcnt vmcnt(0)" : "=&v"(g[0]) : "v"(o[0]), "s"(ws) : "memory");
}
__device__ __forceinline__ void ld_gran2(u32x4 (&g)[2], const char* ws, const u32 (&o)[2]) {
  asm volatile("global_load_dwordx4 %0, %2, %4 sc1\n\tglobal_load_dwordx4 %1, %3, %4 sc1\n\ts_waitcnt vmcnt(0)"
               : "=&v"(g[0]), "=&v"(g[1]) : "v"(o[0]), "v"(o[1]), "s"(ws) : "memory");
}
__device__ __forceinline__ void ld_gran2(u32x4 (&g)[4], const char* ws, const u32 (&o)[4]) {
  asm volatile("global_load_dwordx4 %0, %4, %8 sc1\n\tglobal_load_dwordx4 %1, %5, %8 sc1\n\tglobal_load_dwordx4 %2, %6, %8 sc1\n\tglobal_load_dwordx4 %3, %7, %8 sc1\n\ts_waitcnt vmcnt(0)"
               : "=&v"(g[0]), "=&v"(g[1]), "=&v"(g[2]), "=&v"(g[3]) : "v"(o[0]), "v"(o[1]), "v"(o[2]), "v"(o[3]), "s"(ws) : "memory");
}
template <int I, int N, typename F> __device__ __forceinline__ void static_for(F&& f) {
  if constexpr (I < N) { f(std::integral_constant<int, I>{}); static_for<I + 1, N>(f); }
}
__device__ __forceinline__ void st_gran(char* ws, u32 off, float v, u32 tag) {
  __hip_atomic_store((gu64*)(ws + off), ((u64)tag << 32) | __float_as_uint(v), __ATOMIC_RELAXED, __HIP_MEMORY_SCOPE_AGENT);
}
__device__ __forceinline__ u64 ld_gran(char* ws, u32 off) { return __hip_atomic_load((gu64*)(ws + off), __ATOMIC_RELAXED, __HIP_MEMORY_SCOPE_AGENT); }
__device__ __forceinline__ float gval(u64 g) { return __uint_as_float((u32)g); }
__device__ __forceinline__ u32 gtag(u64 g) { return (u32)(g >> 32); }
template <int N> __device__ __forceinline__ bool tags_ok(const u64 (&g)[N], u32 tag) {
  bool ok = true;
#pragma unroll
  for (int i = 0; i < N; ++i) ok &= gtag(g[i]) == tag;
  return __all(ok);
}

struct Lds {
  float opart[8][192];
  float spart[8][2][96];
  u32 xd[2][128];
  f32x4 ml[4];
  float scale[4];
  u32 qbf[96];
  u32 obf[2][192];
  u32 hbf[2][64];
  float ybuf[2][96];
  u32 flag[8];
  u32 tag;
};
typedef __attribute__((address_space(3))) volatile u32 lvu32;
typedef __attribute__((address_space(3))) volatile u32x4 lvu32x4;

#define TS(i) do { } while (0)

template <int SLEEP = 0, int N, typename F> __device__ __forceinline__ void gather(u64 (&g)[N], char* ws, u32 tag, F off) {
#pragma unroll 1
  for (u32 spin = 0; spin < SPIN_CAP; ++spin) {
#pragma unroll
    for (int i = 0; i < N; ++i) g[i] = ld_gran(ws, off(i));
    if (tags_ok(g, tag)) break;
    if (SLEEP) __builtin_amdgcn_s_sleep(SLEEP);
  }
}
template <int SLEEP = 0, int N, typename F> __device__ __forceinline__ u32 gather2(u32x4 (&g)[N], const char* ws, u32 tag, F off) {
  u32 spin = 0, o[N];
#pragma unroll
  for (int i = 0; i < N; ++i) o[i] = off(i);
#pragma unroll 1
  for (; spin < SPIN_CAP; ++spin) {
    ld_gran2(g, ws, o);
    bool ok = true;
#pragma unroll
    for (int i = 0; i < N; ++i) ok &= (g[i][1] == tag) & (g[i][3] == tag);
    if (__all(ok)) break;
    if (SLEEP) __builtin_amdgcn_s_sleep(SLEEP);
  }
  return spin;
}
__device__ __forceinline__ void lds_set(u32* p) { asm volatile("s_waitcnt lgkmcnt(0)" ::: "memory"); *(lvu32*)p = 1u; }

__global__ void __launch_bounds__(NW * 64, 2) fused_decode(const char* __restrict__ x, const char* __restrict__ K, const char* __restrict__ V,
    const char* __restrict__ wq, const char* __restrict__ wo, const char* __restrict__ wg, const char* __restrict__ wu,
    const char* __restrict__ wd, char* __restrict__ out, char* ws) {
  __shared__ __attribute__((aligned(16))) Lds L;
  TS(0);
  const int tid = threadIdx.x, lane = tid & 63, w = __builtin_amdgcn_readfirstlane(tid >> 6);
  if (tid < 8) L.flag[tid] = 0u;
  if (tid < 4) L.ml[tid] = f32x4{0, 0, 0, 0};
  barrier();
  asm volatile("" :: "s"(x), "s"(K), "s"(V), "s"(wq), "s"(wo), "s"(wg), "s"(wu), "s"(wd), "s"(out), "s"(ws));
  const int r = lane >> 3, c = lane & 7;
  const int xl = blockIdx.x & 7, rk = blockIdx.x >> 3;
  const int h = xl * 4 + (rk >> 3), jj = rk & 7, b = jj >> 2, s = jj & 3;
  const int wgid = (b * H + h) * 4 + s;
  u32 mask[4];
#pragma unroll
  for (int t = 0; t < 4; ++t) mask[t] = ((c >> 1) == t) ? ((c & 1) ? 0xffff0000u : 0x0000ffffu) : 0u;

  const int g4 = lane >> 4, ch = lane & 15;
  u32x4 wor[6];
  const rsrc_t ro = mk(wo + (size_t)(h * DH + w * 24) * (D * 2) + jj * 160, 24 * D * 2 - jj * 160);
  auto issue_wo = [&]() {
    if (LVL < 6) { for (int k = 0; k < 6; ++k) wor[k] = u32x4{0, 0, 0, 0}; return; }
    const int voff = ch < 10 ? g4 * (6 * D * 2) + ch * 16 : OOB;
#pragma unroll
    for (int k = 0; k < 6; ++k) wor[k] = __builtin_amdgcn_raw_buffer_load_b128(ro, voff, k * (D * 2), AUX_WO);
    SB();
  };
  auto out_proj = [&]() {
    const u32* ob = &L.obf[(lane >> 3) & 1][w * 24 + g4 * 6];
    const u32x2 o0 = *(const u32x2*)ob, o1 = *(const u32x2*)(ob + 2), o2 = *(const u32x2*)(ob + 4);
    f32x4 a0 = {0, 0, 0, 0}, a1 = {0, 0, 0, 0};
    a0 = mfma(amask(o0[0], mask), wor[0], a0); a1 = mfma(amask(o0[1], mask), wor[1], a1);
    a0 = mfma(amask(o1[0], mask), wor[2], a0); a1 = mfma(amask(o1[1], mask), wor[3], a1);
    a0 = mfma(amask(o2[0], mask), wor[4], a0); a1 = mfma(amask(o2[1], mask), wor[5], a1);
    if (ch < 10) *(f32x4*)&L.spart[w][lane >> 5][ch * 8 + (g4 & 1) * 4] = a0 + a1;
  };

  if (w >= 4) {
    const int sw = w - 4;
    const rsrc_t rK = mk(K + (size_t)wgid * (1024 * DH * 2), 1024 * DH * 2);
    const rsrc_t rV = mk(V + (size_t)wgid * (1024 * DH * 2), 1024 * DH * 2);
    const int kvoff = r * 384 + c * 16;
    u32x4 kv[64][3];
    auto issue_kv = [&](int j) {
#pragma unroll
      for (int i = 0; i < 3; ++i) kv[j][i] = __builtin_amdgcn_raw_buffer_load_b128(j < 32 ? rK : rV, kvoff + i * 128, ((j & 31) * 4 + sw) * 3072, AUX_KV);
      SB();
    };
#pragma unroll
    for (int j = 0; j < PARK; ++j) {
      if (PACE && j >= RING) { asm volatile("" :: "v"(kv[j - RING][0]), "v"(kv[j - RING][1]), "v"(kv[j - RING][2])); SB(); }
      if (j == 0) TS(2);
      issue_kv(j);
      if (j == 0) TS(3);
    }
    TS(1);
#pragma unroll 1
    for (u32 spin = 0; spin < SPIN_CAP * 16; ++spin) {
      const u32x4 f = *(lvu32x4*)L.flag;
      if (__builtin_amdgcn_readfirstlane(f[1] & f[2] & f[3])) break;
      __builtin_amdgcn_s_sleep(1);
    }
    asm volatile("" ::: "memory");
    u32x4 q[3];
#pragma unroll
    for (int i = 0; i < 3; ++i) q[i] = *(const u32x4*)&L.qbf[i * 32 + c * 4];
    TS(4);
    f32x4 oacc[3] = {{0, 0, 0, 0}, {0, 0, 0, 0}, {0, 0, 0, 0}};
    float sc[32];
    u32 pd[32];
    float mw = -INFINITY, lw = 0.f, mneg = 0.f;
    static_for<0, 64>([&](auto J) {
      constexpr int j = J;
      if constexpr (j < 32) {
        float a0 = 0.f, a1 = 0.f, a2 = 0.f;
#pragma unroll
        for (int t = 0; t < 4; ++t) { a0 = dot2(kv[j][0][t], q[0][t], a0); a1 = dot2(kv[j][1][t], q[1][t], a1); a2 = dot2(kv[j][2][t], q[2][t], a2); }
        float a = (a0 + a1) + a2;
        a += dpp<XOR1>(a); a += dpp<XOR2>(a); a += dpp<HMIR>(a);
        a = rbf(rbf(a) * 0.07216878364870322f);
        asm volatile("" : "+v"(a));
        sc[j] = a; mw = fmaxf(mw, a);
      } else {
        if constexpr (j < 36) {
#pragma unroll
          for (int g = (j - 32) * 8; g < (j - 32) * 8 + 8; ++g) { const float p = __builtin_amdgcn_exp2f(fmaf(sc[g], LOG2E, mneg)); lw += p; pd[g] = dupbf(p); asm volatile("" : "+v"(pd[g])); }
        }
        const u32x4 a = amask(pd[j - 32], mask);
#pragma unroll
        for (int i = 0; i < 3; ++i) oacc[i] = mfma(a, kv[j][i], oacc[i]);
      }
      SB();
      if constexpr (j == 35) {
        lw += dpp<ROR8>(lw); lw = x32_sum(x16_sum(lw));
        if (lane == 0) *(lvu32x4*)&L.ml[sw] = u32x4{__float_as_uint(mw), __float_as_uint(lw), 1u, 0u};
        SB();
      }
      if constexpr (j + RING >= PARK && j + RING < 64) issue_kv(j + RING);
      if constexpr (j == WO_AT) {
        issue_wo();
      }
      if constexpr (j == 31) {
        TS(5);
        mw = fmaxf(mw, dpp<ROR8>(mw)); mw = x16_32_max(mw);
        mneg = -mw * LOG2E;
        SB();
      }
    });
    TS(6);
    if (((lane >> 5) & 1) == ((lane >> 3) & 1)) {
#pragma unroll
      for (int i = 0; i < 3; ++i) *(f32x4*)&L.opart[2 * sw + (lane >> 5)][i * 64 + 8 * c + 4 * (g4 & 1)] = oacc[i];
    }
    barrier();
    barrier();
    out_proj();
    barrier();
  } else {
    const bool b3 = lane & 8;
    const int fcol = 8 * c + 4 * (g4 & 1) + 2 * (lane >> 5);
    const rsrc_t rx = mk(x, 2 * D * 2);
    const rsrc_t rw = mk(ws, WS_END);
    const int li = lane & 31, xo = (lane >> 5) * (D * 2);
    u32x4 xa, xb, xc; u32x2 xs; u32 epoch;
    if (w == 0) {
      xa = __builtin_amdgcn_raw_buffer_load_b128(rx, xo + li * 16, 0, 0);
      xb = __builtin_amdgcn_raw_buffer_load_b128(rx, xo + li * 16, 512, 0);
      xc = __builtin_amdgcn_raw_buffer_load_b128(rx, li < 16 ? xo + li * 16 : OOB, 1024, 0);
      xs = __builtin_amdgcn_raw_buffer_load_b64(rx, li < 20 ? xo + li * 8 : OOB, jj * 160, 0);
      epoch = __hip_atomic_load((gu32*)(ws + WS_EPOCH), __ATOMIC_RELAXED, __HIP_MEMORY_SCOPE_AGENT);
      SB();
    }
    u32x4 wr[10], wr2[10];
    {
      const char* base = w < 3 ? wq + h * (DH * 2) + w * 128 : wg + h * 96;
      const int stride = w < 3 ? H * DH * 2 : F * 2;
      const rsrc_t rq = mk(base, (D - 1) * stride + (w < 3 ? 128 : 96));
      const int voff = (w < 3 || c < 6) ? r * 10 * stride + c * 16 : OOB;
#pragma unroll
      for (int g = 0; g < 10; ++g) wr[g] = __builtin_amdgcn_raw_buffer_load_b128(rq, voff, (jj * 80 + g) * stride, AUX_WQ);
      SB();
      if (w == 3) {
        const rsrc_t ru = mk(wu + h * 96, (D - 1) * F * 2 + 96);
#pragma unroll
        for (int g = 0; g < 10; ++g) wr2[g] = __builtin_amdgcn_raw_buffer_load_b128(ru, voff, (jj * 80 + g) * (F * 2), AUX_WQ);
        SB();
      }
    }
    TS(1);
    if (w == 0) {
      float sum = 0.f;
#pragma unroll
      for (int t = 0; t < 4; ++t) { sum = dot2(xa[t], 0x3f803f80u, sum); sum = dot2(xb[t], 0x3f803f80u, sum); sum = dot2(xc[t], 0x3f803f80u, sum); }
      const float mean = half_sum(sum) * (1.f / D);
      float qa = 0.f, qb = 0.f, qc = 0.f;
#pragma unroll
      for (int t = 0; t < 4; ++t) {
        const float d0 = lo2f(xa[t]) - mean, d1 = hi2f(xa[t]) - mean, d2 = lo2f(xb[t]) - mean, d3 = hi2f(xb[t]) - mean, d4 = lo2f(xc[t]) - mean, d5 = hi2f(xc[t]) - mean;
        qa = fmaf(d0, d0, qa); qa = fmaf(d1, d1, qa); qb = fmaf(d2, d2, qb); qb = fmaf(d3, d3, qb); qc = fmaf(d4, d4, qc); qc = fmaf(d5, d5, qc);
      }
      const float var = half_sum(qa + qb + (li < 16 ? qc : 0.f)) * (1.f / D);
      const float mb = rbf(mean);
      const float rs = __builtin_amdgcn_rcpf(rbf(__builtin_amdgcn_sqrtf(rbf(rbf(var) + 1e-5f))));
      const u32x4 xn = {dupbf(rbf(lo2f(xs[0]) - mb) * rs), dupbf(rbf(hi2f(xs[0]) - mb) * rs), dupbf(rbf(lo2f(xs[1]) - mb) * rs), dupbf(rbf(hi2f(xs[1]) - mb) * rs)};
      *(u32x4*)&L.xd[lane >> 5][li * 4] = xn;
      asm volatile("" : "+v"(epoch));
      L.tag = epoch + 0x9E3779B9u;
      lds_set(&L.flag[0]);
    }
#pragma unroll 1
    for (u32 spin = 0; spin < SPIN_CAP * 16; ++spin) {
      if (__builtin_amdgcn_readfirstlane(*(lvu32*)&L.flag[0])) break;
      __builtin_amdgcn_s_sleep(1);
    }
    asm volatile("" ::: "memory");
    const u32 tag = __builtin_amdgcn_readfirstlane(L.tag);
    {
      u32x2 x0[5], x1[5];
#pragma unroll
      for (int g = 0; g < 5; ++g) { x0[g] = *(const u32x2*)&L.xd[0][r * 10 + g * 2]; x1[g] = *(const u32x2*)&L.xd[1][r * 10 + g * 2]; }
      SB();
      TS(2);
      f32x4 a0 = {0, 0, 0, 0}, a1 = {0, 0, 0, 0};
#pragma unroll
      for (int g = 0; g < 10; ++g) {
        a0 = mfma(amask(x0[g >> 1][g & 1], mask), wr[g], a0);
        a1 = mfma(amask(x1[g >> 1][g & 1], mask), wr[g], a1);
      }
      float e, o;
      fold2(a0, a1, b3, e, o);
      const u32 guo = c < 6 ? WS_GU + ((h * 8 + jj) * 192 + (b3 ? 48 : 0) + fcol) * 8 : (u32)OOB;
      if (LVL < 2) asm volatile("" :: "v"(e), "v"(o)); else
      st_gran2(rw, w < 3 ? WS_Q + (((h * 2 + (b3 ? 1 : 0)) * 8 + jj) * 192 + w * 64 + fcol) * 8 : guo, e, o, tag);
      if (w == 3) {
        a0 = f32x4{0, 0, 0, 0}; a1 = f32x4{0, 0, 0, 0};
#pragma unroll
        for (int g = 0; g < 10; ++g) {
          a0 = mfma(amask(x0[g >> 1][g & 1], mask), wr2[g], a0);
          a1 = mfma(amask(x1[g >> 1][g & 1], mask), wr2[g], a1);
        }
        fold2(a0, a1, b3, e, o);
        if (LVL < 2) asm volatile("" :: "v"(e), "v"(o)); else
        st_gran2(rw, guo + 96 * 8, e, o, tag);
      }
    }
    TS(3);
    if (w < 3) {
      if (DELAY) __builtin_amdgcn_s_sleep(DELAY);
      const u32 go = WS_Q + (((h * 2 + b) * 8 + (lane >> 5)) * 192 + w * 64 + li * 2) * 8;
      u32x4 g[4] = {};
      const u32 spins = LVL < 3 ? 0 : gather2(g, ws, tag, [&](int i) { return go + i * 2 * 192 * 8; });
      const float q0 = x32_sum((__uint_as_float(g[0][0]) + __uint_as_float(g[1][0])) + (__uint_as_float(g[2][0]) + __uint_as_float(g[3][0])));
      const float q1 = x32_sum((__uint_as_float(g[0][2]) + __uint_as_float(g[1][2])) + (__uint_as_float(g[2][2]) + __uint_as_float(g[3][2])));
      const bf16x2 qq = {(__bf16)q0, (__bf16)q1};
      L.qbf[w * 32 + li] = __builtin_bit_cast(u32, qq);
      lds_set(&L.flag[1 + w]);
    }
    TS(5);
    const int idx = h * 4 + s;
    const bool reducer = w == 3 && (idx & 7) < 5;
    const int task = (idx >> 3) * 5 + (idx & 7);
    const u32 roff = ((b * H + r) * D + task * 8 + c) * 8;
    float gate = 0.f, xres = 0.f;
    if (w == 1 && LVL >= 4) {
      __builtin_amdgcn_s_sleep(GLU_DELAY);
      const rsrc_t rd = mk(wd + (size_t)h * (48 * D * 2) + jj * 160, 48 * D * 2 - jj * 160);
      const int c6 = c < 6 ? c : 0, bb = c6 >= 3, pr = c6 - bb * 3;
      u32x4 g[2];
      gather2<8>(g, ws, tag, [&](int i) { return WS_GU + ((h * 8 + r) * 192 + i * 96 + bb * 48 + jj * 6 + pr * 2) * 8; });
      float v[4] = {__uint_as_float(g[0][0]), __uint_as_float(g[0][2]), __uint_as_float(g[1][0]), __uint_as_float(g[1][2])};
#pragma unroll
      for (int i = 0; i < 4; ++i) { v[i] += dpp<ROR8>(v[i]); v[i] = rbf(x32_sum(x16_sum(v[i]))); }
      const bf16x2 hh = {(__bf16)(v[0] * v[2]), (__bf16)(v[1] * v[3])};
      if (lane < 6) st_gran(ws, WS_H + (h * 64 + bb * 24 + jj * 3 + pr) * 8, __builtin_bit_cast(float, hh), tag);
      u32x4 wdr[12];
#pragma unroll
      for (int k = 0; k < 12; ++k) wdr[k] = __builtin_amdgcn_raw_buffer_load_b128(rd, ch < 10 ? g4 * (12 * D * 2) + ch * 16 : OOB, k * (D * 2), AUX_WO);
      __builtin_amdgcn_s_sleep(GLU_DELAY);
      u64 gh[1];
      const int l48 = lane < 48 ? lane : 0;
      gather<16>(gh, ws, tag, [&](int) { return WS_H + (h * 64 + l48) * 8; });
      const u32 hv = (u32)gh[0];
      const int hb = l48 >= 24;
      *(u32x2*)&L.hbf[hb][(l48 - hb * 24) * 2] = u32x2{(hv & 0xffffu) | (hv << 16), (hv & 0xffff0000u) | (hv >> 16)};
      asm volatile("s_waitcnt lgkmcnt(0)" ::: "memory");
      f32x4 y = {0, 0, 0, 0};
#pragma unroll
      for (int k = 0; k < 12; ++k) y = mfma(amask(L.hbf[(lane >> 3) & 1][g4 * 12 + k], mask), wdr[k], y);
      if (ch < 10) *(f32x4*)&L.ybuf[lane >> 5][ch * 8 + (g4 & 1) * 4] = y;
      asm volatile("s_waitcnt lgkmcnt(0)" ::: "memory");
      if (lane < 40) {
#pragma unroll
        for (int t = 0; t < 2; ++t) st_gran2(rw, WS_G2Y + ((t * H + h) * D + jj * 80 + lane * 2) * 8, L.ybuf[t][lane * 2], L.ybuf[t][lane * 2 + 1], tag);
      }
    }
    issue_wo();
    if (w == 0 || reducer) {
#pragma unroll 1
      for (u32 spin = 0; spin < SPIN_CAP * 16; ++spin) {
        const u32 f = ((lvu32*)L.ml)[(lane & 3) * 4 + 2];
        if (__all(f != 0u)) break;
        __builtin_amdgcn_s_sleep(4);
      }
      asm volatile("" ::: "memory");
    }
    if (w == 0 && LVL >= 5) {
      const f32x4 mine = L.ml[lane & 3];
      float M = mine[0];
      M = fmaxf(M, dpp<XOR1>(M)); M = fmaxf(M, dpp<XOR2>(M));
      float l = mine[1] * __builtin_amdgcn_exp2f((mine[0] - M) * LOG2E);
      l += dpp<XOR1>(l); l += dpp<XOR2>(l);
      st_gran2(rw, lane == 0 ? WS_ML + wgid * 128 : (u32)OOB, M, l, tag);
      u32x4 g[1];
      gather2<16>(g, ws, tag, [&](int) { return WS_ML + ((wgid & ~3) + (lane & 3)) * 128; });
      float MM = __uint_as_float(g[0][0]);
      MM = fmaxf(MM, dpp<XOR1>(MM)); MM = fmaxf(MM, dpp<XOR2>(MM));
      float LL = __uint_as_float(g[0][2]) * __builtin_amdgcn_exp2f((__uint_as_float(g[0][0]) - MM) * LOG2E);
      LL += dpp<XOR1>(LL); LL += dpp<XOR2>(LL);
      if (lane < 4) L.scale[lane] = __builtin_amdgcn_exp2f((mine[0] - MM) * LOG2E) * __builtin_amdgcn_rcpf(LL);
    } else if (reducer && LVL >= 4) {
      xres = lo2f(__builtin_amdgcn_raw_buffer_load_b16(rx, (b * D + task * 8 + c) * 2, 0, 0));
      u64 g[4];
      gather<16>(g, ws, tag, [&](int i) { return WS_G2Y + roff + i * 8 * D * 8; });
      float yv = (gval(g[0]) + gval(g[1])) + (gval(g[2]) + gval(g[3]));
      yv += dpp<ROR8>(yv);
      yv = rbf(x32_sum(x16_sum(yv)));
      gate = rbf(yv * __builtin_amdgcn_rcpf(1.f + __builtin_amdgcn_exp2f(-yv * LOG2E)));
    }
    TS(6);
    barrier();
    TS(7);
    if (w < 3 && LVL >= 7) {
      float op[8];
      const f32x4 sc4 = *(const f32x4*)L.scale;
#pragma unroll
      for (int i = 0; i < 8; ++i) op[i] = L.opart[i][tid];
      SB();
      const float o = (sc4[0] * (op[0] + op[1]) + sc4[1] * (op[2] + op[3])) + (sc4[2] * (op[4] + op[5]) + sc4[3] * (op[6] + op[7]));
      st_gran(ws, WS_G1 + wgid * 1536 + tid * 8, o, tag);
      TS(8);
      u64 g[7];
      gather<TSLEEP>(g, ws, tag, [&](int i) { return WS_G1 + (i < 3 ? ((wgid & ~3) | ((s + 1 + i) & 3)) : (((wgid & ~3) ^ (H * 4)) + i - 3)) * 1536 + tid * 8; });
      TS(9);
      L.obf[b][tid] = dupbf((o + gval(g[0])) + (gval(g[1]) + gval(g[2])));
      L.obf[b ^ 1][tid] = dupbf((gval(g[3]) + gval(g[4])) + (gval(g[5]) + gval(g[6])));
    }
    barrier();
    TS(10);
    out_proj();
    barrier();
    TS(11);
    if (tid < 160 && LVL >= 7) {
      const int bb = tid >= 80, cc = tid - bb * 80;
      float sp[8];
#pragma unroll
      for (int p = 0; p < 8; ++p) sp[p] = L.spart[p][bb][cc];
      SB();
      st_gran(ws, WS_G2S + ((bb * H + h) * D + jj * 80 + cc) * 8, ((sp[0] + sp[1]) + (sp[2] + sp[3])) + ((sp[4] + sp[5]) + (sp[6] + sp[7])), tag);
    }
    TS(12);
    if (reducer && LVL >= 7) {
      u64 g[4];
      gather<TSLEEP>(g, ws, tag, [&](int i) { return WS_G2S + roff + i * 8 * D * 8; });
      TS(13);
      float sv = (gval(g[0]) + gval(g[1])) + (gval(g[2]) + gval(g[3]));
      sv += dpp<ROR8>(sv);
      sv = rbf(x32_sum(x16_sum(sv)));
      const float res = xres + rbf(sv * gate);
      if (lane < 8) ((unsigned short*)out)[b * D + task * 8 + c] = (unsigned short)dupbf(res);
      if (blockIdx.x == 0 && lane == 0) __hip_atomic_store((gu32*)(ws + WS_EPOCH), tag, __ATOMIC_RELAXED, __HIP_MEMORY_SCOPE_AGENT);
    }
  }
}
}

extern "C" void kernel_launch(void* const* d_in, const int* in_sizes, int n_in, void* d_out, int out_size, void* d_ws, size_t ws_size, hipStream_t stream) {
  fused_decode<<<256, NW * 64, 0, stream>>>((const char*)d_in[0], (const char*)d_in[1], (const char*)d_in[2], (const char*)d_in[3], (const char*)d_in[4],
                                            (const char*)d_in[6], (const char*)d_in[7], (const char*)d_in[8], (char*)d_out, (char*)d_ws);
}
```

```cpp
#define TAU 450
#define T0 2300
#include <hip/hip_runtime.h>
#include <hip/hip_bf16.h>
#include <stdint.h>
#include <type_traits>

#define AUX_KV 2
#define AUX_WQ 2
#define AUX_WO 2
#define AUX_WD 2
#define PARK 14
#define RING 3
#define CATCH 4
#define DELAY 64
#define GLU_DELAY 127
#define TSLEEP 0
#define RMAP 1
#define XO 4
#define XQ 4
#define WOS 0
#define WOA 0
#define ML_DELAY 127
#define WO_AT (63 - RING)

namespace {
constexpr int D = 640, H = 32, DH = 192, F = 1536;
constexpr int NW = 8;
constexpr float LOG2E = 1.4426950408889634f;
typedef unsigned u32;
typedef unsigned long long u64;
typedef u32 u32x2 __attribute__((ext_vector_type(2)));
typedef u32 u32x4 __attribute__((ext_vector_type(4)));
typedef float f32x2 __attribute__((ext_vector_type(2)));
typedef float f32x4 __attribute__((ext_vector_type(4)));
typedef __bf16 bf16x2 __attribute__((ext_vector_type(2)));
typedef __bf16 bf16x8 __attribute__((ext_vector_type(8)));
typedef __amdgpu_buffer_rsrc_t rsrc_t;
typedef __attribute__((address_space(1))) u32 gu32;
typedef __attribute__((address_space(3))) volatile u32 lvu32;
typedef __attribute__((address_space(3))) volatile u32x4 lvu32x4;
#define SB() __builtin_amdgcn_sched_barrier(0)

constexpr u32 WS_EPOCH = 0;
constexpr u32 WS_Q = 4096;
constexpr u32 WS_GU = WS_Q + 32 * 2 * 2 * 192 * 8;
constexpr u32 WS_H = WS_GU + 8 * 8 * 2 * 192 * 8;
constexpr u32 WS_Y = WS_H + 8 * 192 * 8;
constexpr u32 WS_ML = WS_Y + 2 * 16 * 640 * 8;
constexpr u32 WS_O = WS_ML + 256 * 16 * 8;
constexpr u32 WS_S = WS_O + 8 * 8 * 768 * 8;
constexpr u32 WS_END = WS_S + 2 * 24 * 640 * 8;
constexpr u32 WS_STAMP = 4u << 20;
constexpr u32 SPIN_CAP = 1u << 20;
constexpr u32 OOB = 0x7ffffff0u;

__device__ __forceinline__ rsrc_t mk(const void* p, u32 bytes) { return __builtin_amdgcn_make_buffer_rsrc(const_cast<void*>(p), 0, (int)bytes, 0x00020000); }
__device__ __forceinline__ float dot2(u32 a, u32 b, float c) { return __builtin_amdgcn_fdot2_f32_bf16(__builtin_bit_cast(bf16x2, a), __builtin_bit_cast(bf16x2, b), c, false); }
__device__ __forceinline__ f32x4 mfma(u32x4 a, u32x4 b, f32x4 c) { return __builtin_amdgcn_mfma_f32_16x16x32_bf16(__builtin_bit_cast(bf16x8, a), __builtin_bit_cast(bf16x8, b), c, 0, 0, 0); }
template <int C> __device__ __forceinline__ u32 dppu(u32 x) { return (u32)__builtin_amdgcn_mov_dpp((int)x, C, 0xf, 0xf, true); }
template <int C> __device__ __forceinline__ float dpp(float x) { return __uint_as_float(dppu<C>(__float_as_uint(x))); }
constexpr int XOR1 = 0xB1, XOR2 = 0x4E, HMIR = 0x141, ROR8 = 0x128;
__device__ __forceinline__ float lo2f(u32 v) { return __uint_as_float(v << 16); }
__device__ __forceinline__ float hi2f(u32 v) { return __uint_as_float(v & 0xffff0000u); }
__device__ __forceinline__ float rbf(float f) { return (float)(__bf16)f; }
__device__ __forceinline__ u32 pkbf(float a, float b) { bf16x2 v = {(__bf16)a, (__bf16)b}; return __builtin_bit_cast(u32, v); }
__device__ __forceinline__ u32 dupbf(float f) { return pkbf(f, f); }
__device__ __forceinline__ float x16_sum(float x) {
  auto s = __builtin_amdgcn_permlane16_swap(__float_as_uint(x), __float_as_uint(x), false, false);
  return __uint_as_float(s[0]) + __uint_as_float(s[1]);
}
__device__ __forceinline__ float x32_sum(float x) {
  auto t = __builtin_amdgcn_permlane32_swap(__float_as_uint(x), __float_as_uint(x), false, false);
  return __uint_as_float(t[0]) + __uint_as_float(t[1]);
}
__device__ __forceinline__ float x16_32_max(float x) {
  auto s = __builtin_amdgcn_permlane16_swap(__float_as_uint(x), __float_as_uint(x), false, false);
  x = fmaxf(__uint_as_float(s[0]), __uint_as_float(s[1]));
  auto t = __builtin_amdgcn_permlane32_swap(__float_as_uint(x), __float_as_uint(x), false, false);
  return fmaxf(__uint_as_float(t[0]), __uint_as_float(t[1]));
}
__device__ __forceinline__ float half_sum(float x) { x += dpp<XOR1>(x); x += dpp<XOR2>(x); x += dpp<HMIR>(x); x += dpp<ROR8>(x); return x16_sum(x); }
__device__ __forceinline__ float col_sum(float x) { x += dpp<ROR8>(x); return x32_sum(x16_sum(x)); }
__device__ __forceinline__ void barrier() { asm volatile("s_waitcnt lgkmcnt(0)\n\ts_barrier" ::: "memory"); }
__device__ __forceinline__ u32x4 amask(u32 dup, const u32 (&m)[4]) { return u32x4{dup & m[0], dup & m[1], dup & m[2], dup & m[3]}; }
__device__ __forceinline__ f32x2 fold2(f32x4 a, f32x4 b, bool b3) {
  auto p = __builtin_amdgcn_permlane32_swap(__float_as_uint(a[0]), __float_as_uint(a[2]), false, false);
  auto q = __builtin_amdgcn_permlane32_swap(__float_as_uint(a[1]), __float_as_uint(a[3]), false, false);
  auto u = __builtin_amdgcn_permlane32_swap(__float_as_uint(b[0]), __float_as_uint(b[2]), false, false);
  auto v = __builtin_amdgcn_permlane32_swap(__float_as_uint(b[1]), __float_as_uint(b[3]), false, false);
  const float e0 = __uint_as_float(p[0]) + dpp<ROR8>(__uint_as_float(p[1])), o0 = __uint_as_float(q[0]) + dpp<ROR8>(__uint_as_float(q[1]));
  const float e1 = __uint_as_float(u[1]) + dpp<ROR8>(__uint_as_float(u[0])), o1 = __uint_as_float(v[1]) + dpp<ROR8>(__uint_as_float(v[0]));
  return f32x2{b3 ? e1 : e0, b3 ? o1 : o0};
}
template <int I, int N, typename F> __device__ __forceinline__ void static_for(F&& f) {
  if constexpr (I < N) { f(std::integral_constant<int, I>{}); static_for<I + 1, N>(f); }
}

__device__ __forceinline__ void st1(rsrc_t rw, u32 off, u32 v, u32 tag) { __builtin_amdgcn_raw_buffer_store_b64(u32x2{v, tag}, rw, off, 0, 16); }
__device__ __forceinline__ void st2(rsrc_t rw, u32 off, f32x2 v, u32 tag) {
  __builtin_amdgcn_raw_buffer_store_b128(u32x4{__float_as_uint(v[0]), tag, __float_as_uint(v[1]), tag}, rw, off, 0, 16);
}
template <int N> __device__ __forceinline__ void ld1(u32x2 (&g)[N], u32x4 rw, const u32 (&o)[N]) {
  if constexpr (N == 2) asm volatile("buffer_load_dwordx2 %0, %2, %4, 0 offen sc1\n\tbuffer_load_dwordx2 %1, %3, %4, 0 offen sc1\n\ts_waitcnt vmcnt(0)"
                                     : "=&v"(g[0]), "=&v"(g[1]) : "v"(o[0]), "v"(o[1]), "s"(rw) : "memory");
  if constexpr (N == 3) asm volatile("buffer_load_dwordx2 %0, %3, %6, 0 offen sc1\n\tbuffer_load_dwordx2 %1, %4, %6, 0 offen sc1\n\tbuffer_load_dwordx2 %2, %5, %6, 0 offen sc1\n\ts_waitcnt vmcnt(0)"
                                     : "=&v"(g[0]), "=&v"(g[1]), "=&v"(g[2]) : "v"(o[0]), "v"(o[1]), "v"(o[2]), "s"(rw) : "memory");
}
template <int N> __device__ __forceinline__ void ld2(u32x4 (&g)[N], u32x4 rw, const u32 (&o)[N]) {
  if constexpr (N == 1) asm volatile("buffer_load_dwordx4 %0, %1, %2, 0 offen sc1\n\ts_waitcnt vmcnt(0)" : "=&v"(g[0]) : "v"(o[0]), "s"(rw) : "memory");
  if constexpr (N == 4) asm volatile("buffer_load_dwordx4 %0, %4, %8, 0 offen sc1\n\tbuffer_load_dwordx4 %1, %5, %8, 0 offen sc1\n\tbuffer_load_dwordx4 %2, %6, %8, 0 offen sc1\n\t"
                                     "buffer_load_dwordx4 %3, %7, %8, 0 offen sc1\n\ts_waitcnt vmcnt(0)"
                                     : "=&v"(g[0]), "=&v"(g[1]), "=&v"(g[2]), "=&v"(g[3]) : "v"(o[0]), "v"(o[1]), "v"(o[2]), "v"(o[3]), "s"(rw) : "memory");
}
template <int N, int SLEEP> __device__ __forceinline__ void wait1(u32 (&val)[N], u32x4 rw, u32 off, u32 stride, u32 tag) {
  u32 o[N];
#pragma unroll
  for (int i = 0; i < N; ++i) { val[i] = 0u; o[i] = off + i * stride; }
#pragma unroll 1
  for (u32 spin = 0; spin < SPIN_CAP; ++spin) {
    u32x2 g[N];
    ld1<N>(g, rw, o);
    u32 left = OOB;
#pragma unroll
    for (int i = 0; i < N; ++i) { const bool hit = g[i][1] == tag; val[i] = hit ? g[i][0] : val[i]; o[i] = hit ? OOB : o[i]; left = min(left, o[i]); }
    if (__all(left == OOB)) break;
    if (SLEEP) __builtin_amdgcn_s_sleep(SLEEP);
  }
}
template <int N, int SLEEP> __device__ __forceinline__ u32 wait2(f32x2 (&val)[N], u32x4 rw, u32 off, u32 stride, u32 tag) {
  u32 o[N], spin = 0;
#pragma unroll
  for (int i = 0; i < N; ++i) { val[i] = f32x2{0.f, 0.f}; o[i] = off + i * stride; }
#pragma unroll 1
  for (; spin < SPIN_CAP; ++spin) {
    u32x4 g[N];
    ld2<N>(g, rw, o);
    u32 left = OOB;
#pragma unroll
    for (int i = 0; i < N; ++i) {
      const bool hit = (g[i][1] == tag) & (g[i][3] == tag);
      val[i][0] = hit ? __uint_as_float(g[i][0]) : val[i][0]; val[i][1] = hit ? __uint_as_float(g[i][2]) : val[i][1];
      o[i] = hit ? OOB : o[i]; left = min(left, o[i]);
    }
    if (__all(left == OOB)) break;
    if (SLEEP) __builtin_amdgcn_s_sleep(SLEEP);
  }
  return spin;
}

struct Lds {
  float opart[8][192];
  f32x2 red[4][3][64];
  f32x2 sred[8][64];
  u32 xd[4][2][128];
  f32x4 ml[4];
  float scale[4];
  u32 qbf[96];
  u32 hbf[2][128];
  u32 obf[2][256];
  u32 flag[12];
};
__device__ __forceinline__ void lds_set(u32* p, u32 v) { asm volatile("s_waitcnt lgkmcnt(0)" ::: "memory"); *(lvu32*)p = v; }
template <int SLEEP> __device__ __forceinline__ u32x4 lds_wait4(const u32* p, u32 ignore0) {
  u32x4 f;
#pragma unroll 1
  for (u32 spin = 0; spin < SPIN_CAP * 16; ++spin) {
    f = *(lvu32x4*)p;
    if (__builtin_amdgcn_readfirstlane((f[0] | ignore0) & f[1] & f[2] & f[3] & 1u)) break;
    __builtin_amdgcn_s_sleep(SLEEP);
  }
  asm volatile("" ::: "memory");
  return f;
}

#define TS(i) do { } while (0)

constexpr int issued(int j) {
  const int a = PARK + (j + 1) / CATCH, b = j + 1 + RING, m = a > b ? a : b;
  return m < 64 ? m : 64;
}

__global__ void __launch_bounds__(NW * 64, 2) fused_decode(const char* __restrict__ x, const char* __restrict__ K, const char* __restrict__ V,
    const char* __restrict__ wq, const char* __restrict__ wo, const char* __restrict__ wg, const char* __restrict__ wu,
    const char* __restrict__ wd, char* __restrict__ out, char* ws) {
  __shared__ __attribute__((aligned(16))) Lds L;
  TS(0);
  u32 lk0; { u64 t_ = __builtin_amdgcn_s_memrealtime(); asm volatile("" : "+s"(t_)); lk0 = (u32)t_; }
  const int tid = threadIdx.x, lane = tid & 63, w = __builtin_amdgcn_readfirstlane(tid >> 6);
  if (tid < 12) L.flag[tid] = 0u;
  if (tid < 4) L.ml[tid] = f32x4{0, 0, 0, 0};
  barrier();
  asm volatile("" :: "s"(x), "s"(K), "s"(V), "s"(wq), "s"(wo), "s"(wg), "s"(wu), "s"(wd), "s"(out), "s"(ws));
  const int r = lane >> 3, c = lane & 7, g4 = lane >> 4;
  const int xl = blockIdx.x & 7, rk = blockIdx.x >> 3;
  const int h = xl * 4 + (rk >> 3), jj = rk & 7, b = jj >> 2, s = jj & 3;
  const int wgid = (b * H + h) * 4 + s;
  const bool b3 = lane & 8;
  u32 mask[4];
#pragma unroll
  for (int t = 0; t < 4; ++t) mask[t] = ((c >> 1) == t) ? ((c & 1) ? 0xffff0000u : 0x0000ffffu) : 0u;
  const rsrc_t rw = mk(ws, WS_END);
  const u32x4 rws = {(u32)(uintptr_t)ws, (u32)((uintptr_t)ws >> 32) & 0xffffu, WS_END, 0x00020000u};

  const bool otask = rk < 30;
  const int t3 = (rk >= 10) + (rk >= 20), cb0 = rk - t3 * 10;
  const bool swp = (cb0 & 7) == xl;
  const int xt = xl ^ (swp ? XO ^ 1 : XO), cb = cb0 ^ (swp ? 1 : 0);
  u32x4 wor[4];
  const rsrc_t ro = mk(wo + (size_t)(xt * 768 + t3 * 256 + w * 32) * (D * 2) + (otask ? cb * 128 : 0), otask ? 31 * D * 2 + 128 : 0);
  auto issue_wo = [&](bool wait) {
    if (wait) lds_wait4<1>(&L.flag[4], 0u);
#pragma unroll
    for (int k = 0; k < 4; ++k) wor[k] = __builtin_amdgcn_raw_buffer_load_b128(ro, r * (4 * D * 2) + c * 16, k * (D * 2), AUX_WO);
    SB();
  };
  auto out_proj = [&]() {
    const u32x4 o0 = *(const u32x4*)&L.obf[0][w * 32 + r * 4], o1 = *(const u32x4*)&L.obf[1][w * 32 + r * 4];
    f32x4 a0 = {0, 0, 0, 0}, a1 = {0, 0, 0, 0};
#pragma unroll
    for (int k = 0; k < 4; ++k) { a0 = mfma(amask(o0[k], mask), wor[k], a0); a1 = mfma(amask(o1[k], mask), wor[k], a1); }
    L.sred[w][lane] = fold2(a0, a1, b3);
  };

  if (w >= 4) {
    const int sw = w - 4;
    const rsrc_t rK = mk(K + (size_t)wgid * (1024 * DH * 2), 1024 * DH * 2);
    const rsrc_t rV = mk(V + (size_t)wgid * (1024 * DH * 2), 1024 * DH * 2);
    const int kvoff = r * 384 + c * 16;
    u32x4 kv[64][3];
    const u32 tbase = (u32)__builtin_amdgcn_s_memrealtime();
    auto issue_kv = [&](int j) {
      if (j >= RING) { const int tj = (T0 + (j - RING) * TAU) / 10; while ((int)((u32)__builtin_amdgcn_s_memrealtime() - tbase) < tj) __builtin_amdgcn_s_sleep(1); SB(); }
#pragma unroll
      for (int i = 0; i < 3; ++i) kv[j][i] = __builtin_amdgcn_raw_buffer_load_b128(j < 32 ? rK : rV, kvoff + i * 128, ((j & 31) * 4 + sw) * 3072, AUX_KV);
      SB();
    };
    TS(2);
#pragma unroll
    for (int j = 0; j < PARK; ++j) {
      if (j >= RING) { asm volatile("" :: "v"(kv[j - RING][0]), "v"(kv[j - RING][1]), "v"(kv[j - RING][2])); SB(); }
      issue_kv(j);
    }
    TS(1);
    const u32 tag = __builtin_amdgcn_readfirstlane(lds_wait4<1>(L.flag, 0u)[0]);
    u32x4 q[3];
#pragma unroll
    for (int i = 0; i < 3; ++i) q[i] = *(const u32x4*)&L.qbf[i * 32 + c * 4];
    TS(4);
    f32x4 oacc[3] = {{0, 0, 0, 0}, {0, 0, 0, 0}, {0, 0, 0, 0}};
    float sc[32];
    u32 pd[32];
    float mw = -INFINITY, lw = 0.f, mneg = 0.f;
    static_for<0, 64>([&](auto J) {
      constexpr int j = J;
      if constexpr (j < 32) {
        float a0 = 0.f, a1 = 0.f, a2 = 0.f;
#pragma unroll
        for (int t = 0; t < 4; ++t) { a0 = dot2(kv[j][0][t], q[0][t], a0); a1 = dot2(kv[j][1][t], q[1][t], a1); a2 = dot2(kv[j][2][t], q[2][t], a2); }
        float a = (a0 + a1) + a2;
        a += dpp<XOR1>(a); a += dpp<XOR2>(a); a += dpp<HMIR>(a);
        a = rbf(rbf(a) * 0.07216878364870322f);
        asm volatile("" : "+v"(a));
        sc[j] = a; mw = fmaxf(mw, a);
      } else {
        const u32x4 a = amask(pd[j - 32], mask);
#pragma unroll
        for (int i = 0; i < 3; ++i) oacc[i] = mfma(a, kv[j][i], oacc[i]);
      }
      SB();
#pragma unroll
      for (int n = (j ? issued(j - 1) : PARK); n < issued(j); ++n) issue_kv(n);
      if constexpr (j == 57) TS(3);
      if constexpr (j == WO_AT) { *(lvu32*)&L.flag[4 + sw] = 1u; if (WOS == 0) issue_wo(true); }
      if constexpr (j == 31) {
        TS(5);
        mw = fmaxf(mw, dpp<ROR8>(mw)); mw = x16_32_max(mw);
        mneg = -mw * LOG2E;
      }
      if constexpr (j >= 31 && j < 35) {
#pragma unroll
        for (int g = (j - 31) * 8; g < (j - 31) * 8 + 8; ++g) { const float p = __builtin_amdgcn_exp2f(fmaf(sc[g], LOG2E, mneg)); lw += p; pd[g] = dupbf(p); asm volatile("" : "+v"(pd[g])); }
        if constexpr (j == 34) {
          lw = col_sum(lw);
          if (lane == 0) *(lvu32x4*)&L.ml[sw] = u32x4{__float_as_uint(mw), __float_as_uint(lw), 1u, 0u};
        }
        SB();
      }
    });
    TS(6);
    if (((lane >> 5) & 1) == ((lane >> 3) & 1)) {
#pragma unroll
      for (int i = 0; i < 3; ++i) *(f32x4*)&L.opart[2 * sw + (lane >> 5)][i * 64 + 8 * c + 4 * (g4 & 1)] = oacc[i];
    }
    const u32 ooff = WS_O + ((xt * 2 + (sw >> 1)) * 4 * 768 + t3 * 256 + (sw & 1) * 128 + lane * 2) * 8;
    barrier();
    TS(7);
    if (WOS == 1) issue_wo(false);
    if (otask) {
      f32x2 g[4];
      wait2<4, TSLEEP>(g, rws, ooff, 768 * 8, tag);
      TS(9);
      const f32x2 o = (g[0] + g[1]) + (g[2] + g[3]);
      *(u32x2*)&L.obf[sw >> 1][(sw & 1) * 128 + lane * 2] = u32x2{dupbf(o[0]), dupbf(o[1])};
      barrier();
      TS(10);
      out_proj();
      barrier();
    }
  } else {
    const int fcol = 8 * c + 4 * (g4 & 1) + 2 * (lane >> 5);
    const rsrc_t rx = mk(x, 2 * D * 2);
    const bool glu = jj >= 6;
    const int hh = rk >> 3, kh = jj >= 3, blk = jj - 3 * kh;
    const int hq = (xl ^ XQ) * 4 + hh;
    const int row0 = glu ? hh * 160 + w * 40 : kh * 320 + w * 80;
    const int li = lane & 31, xo = (lane >> 5) * (D * 2);
    TS(4);
    const u32x4 xa = __builtin_amdgcn_raw_buffer_load_b128(rx, xo + li * 16, 0, 0);
    const u32x4 xb = __builtin_amdgcn_raw_buffer_load_b128(rx, xo + li * 16, 512, 0);
    const u32x4 xc = __builtin_amdgcn_raw_buffer_load_b128(rx, li < 16 ? xo + li * 16 : OOB, 1024, 0);
    const u32x2 xs = __builtin_amdgcn_raw_buffer_load_b64(rx, li < 20 ? xo + li * 8 : OOB, row0 * 2, 0);
    u32 epoch = __hip_atomic_load((gu32*)(ws + WS_EPOCH), __ATOMIC_RELAXED, __HIP_MEMORY_SCOPE_AGENT);
    SB();
    u32x4 wr[15];
    {
      const int stride = glu ? F * 2 : H * DH * 2;
      const char* base = (glu ? (jj == 6 ? wg : wu) + xl * 384 : wq + hq * (DH * 2) + blk * 128) + (size_t)row0 * stride;
      const rsrc_t rq = mk(base, glu ? 39 * stride + 384 : 79 * stride + 128);
      const u32 voff = r * (glu ? 5 : 10) * stride + c * 16;
      const int step5 = glu ? 128 : 5 * stride;
#pragma unroll
      for (int n = 0; n < 10; ++n) wr[n] = __builtin_amdgcn_raw_buffer_load_b128(rq, voff, (n % 5) * stride + (n / 5) * step5, AUX_WQ);
      SB();
      if (glu) {
#pragma unroll
        for (int n = 10; n < 15; ++n) wr[n] = __builtin_amdgcn_raw_buffer_load_b128(rq, voff, (n % 5) * stride + 256, AUX_WQ);
        SB();
      }
    }
    TS(1);
    const u32 tag = (__builtin_amdgcn_readfirstlane(epoch) + 0x9E3779B8u) | 1u;
    {
      float sum = 0.f;
#pragma unroll
      for (int t = 0; t < 4; ++t) { sum = dot2(xa[t], 0x3f803f80u, sum); sum = dot2(xb[t], 0x3f803f80u, sum); sum = dot2(xc[t], 0x3f803f80u, sum); }
      const float mean = half_sum(sum) * (1.f / D);
      float qa = 0.f, qb = 0.f, qc = 0.f;
#pragma unroll
      for (int t = 0; t < 4; ++t) {
        const float d0 = lo2f(xa[t]) - mean, d1 = hi2f(xa[t]) - mean, d2 = lo2f(xb[t]) - mean, d3 = hi2f(xb[t]) - mean, d4 = lo2f(xc[t]) - mean, d5 = hi2f(xc[t]) - mean;
        qa = fmaf(d0, d0, qa); qa = fmaf(d1, d1, qa); qb = fmaf(d2, d2, qb); qb = fmaf(d3, d3, qb); qc = fmaf(d4, d4, qc); qc = fmaf(d5, d5, qc);
      }
      const float var = half_sum(qa + qb + (li < 16 ? qc : 0.f)) * (1.f / D);
      const float mb = rbf(mean);
      const float rs = __builtin_amdgcn_rcpf(rbf(__builtin_amdgcn_sqrtf(rbf(rbf(var) + 1e-5f))));
      *(u32x4*)&L.xd[w][lane >> 5][li * 4] = u32x4{dupbf(rbf(lo2f(xs[0]) - mb) * rs), dupbf(rbf(hi2f(xs[0]) - mb) * rs), dupbf(rbf(lo2f(xs[1]) - mb) * rs), dupbf(rbf(hi2f(xs[1]) - mb) * rs)};
      asm volatile("s_waitcnt lgkmcnt(0)" ::: "memory");
      TS(2);
      const f32x4 zero = {0, 0, 0, 0};
      if (glu) {
        f32x4 a0[3] = {zero, zero, zero}, a1[3] = {zero, zero, zero};
#pragma unroll
        for (int g = 0; g < 5; ++g) {
          const u32x4 m0 = amask(L.xd[w][0][r * 5 + g], mask), m1 = amask(L.xd[w][1][r * 5 + g], mask);
#pragma unroll
          for (int i = 0; i < 3; ++i) { a0[i] = mfma(m0, wr[i * 5 + g], a0[i]); a1[i] = mfma(m1, wr[i * 5 + g], a1[i]); }
        }
#pragma unroll
        for (int i = 0; i < 3; ++i) L.red[w][i][lane] = fold2(a0[i], a1[i], b3);
      } else {
        u32x2 x0[5], x1[5];
#pragma unroll
        for (int g = 0; g < 5; ++g) { x0[g] = *(const u32x2*)&L.xd[w][0][r * 10 + g * 2]; x1[g] = *(const u32x2*)&L.xd[w][1][r * 10 + g * 2]; }
        SB();
        f32x4 a0 = zero, a1 = zero;
#pragma unroll
        for (int g = 0; g < 10; ++g) {
          a0 = mfma(amask(x0[g >> 1][g & 1], mask), wr[g], a0);
          a1 = mfma(amask(x1[g >> 1][g & 1], mask), wr[g], a1);
        }
        L.red[w][0][lane] = fold2(a0, a1, b3);
      }
      lds_set(&L.flag[8 + w], 1u);
      if (w == 0) {
        *(lvu32*)&L.flag[0] = tag;
        lds_wait4<1>(&L.flag[8], 0u);
        const u32 po = glu ? WS_GU + ((((xl * 2 + jj - 6) * 4 + hh) * 2 + (b3 ? 1 : 0)) * 192 + fcol) * 8
                           : WS_Q + (((hq * 2 + (b3 ? 1 : 0)) * 2 + kh) * 192 + blk * 64 + fcol) * 8;
        st2(rw, po, (L.red[0][0][lane] + L.red[1][0][lane]) + (L.red[2][0][lane] + L.red[3][0][lane]), tag);
        if (glu) {
#pragma unroll
          for (int i = 1; i < 3; ++i) st2(rw, po + i * 512, (L.red[0][i][lane] + L.red[1][i][lane]) + (L.red[2][i][lane] + L.red[3][i][lane]), tag);
        }
      }
    }
    TS(3);
    if (w != 0) {
      if (!glu) __builtin_amdgcn_s_sleep(DELAY); else __builtin_amdgcn_s_sleep(DELAY / 2);
      f32x2 g[1];
      const u32 spins = wait2<1, 8>(g, rws, WS_Q + (((h * 2 + b) * 2 + (lane >> 5)) * 192 + (w - 1) * 64 + li * 2) * 8, 0, tag);
      L.qbf[(w - 1) * 32 + li] = pkbf(x32_sum(g[0][0]), x32_sum(g[0][1]));
      lds_set(&L.flag[w], 1u);
    }
    TS(5);
    if (w == 2 && lane == 0) { __hip_atomic_store((gu32*)(ws + WS_STAMP + blockIdx.x * 4), lk0, __ATOMIC_RELAXED, __HIP_MEMORY_SCOPE_AGENT); __hip_atomic_store((gu32*)(ws + WS_STAMP + 2048 + blockIdx.x * 4), (u32)__builtin_amdgcn_s_memrealtime(), __ATOMIC_RELAXED, __HIP_MEMORY_SCOPE_AGENT); }
    const bool reducer = w == 3 && (rk < 16 || xl < 2);
    const int task = (xl + (rk & 16) / 2) * 8 + (rk & 7), rb = (rk >> 3) & 1;
    float gate = 0.f, xres = 0.f;
    const int ri = hh * 6 + jj;
    if (w == 1 && !glu && ri < 23) {
      __builtin_amdgcn_s_sleep(GLU_DELAY);
      if (ri >= 20) {
        const u32 go = WS_GU + ((xl * 16 + (lane >> 5)) * 192 + (ri - 20) * 64 + li * 2) * 8;
        f32x2 g[4], u[4];
        wait2<4, 32>(g, rws, go, 2 * 192 * 8, tag);
        wait2<4, 32>(u, rws, go + 8 * 192 * 8, 2 * 192 * 8, tag);
        const f32x2 gs = (g[0] + g[1]) + (g[2] + g[3]), us = (u[0] + u[1]) + (u[2] + u[3]);
        st1(rw, WS_H + ((xl * 2 + (lane >> 5)) * 96 + (ri - 20) * 32 + li) * 8, pkbf(rbf(gs[0]) * rbf(us[0]), rbf(gs[1]) * rbf(us[1])), tag);
      } else {
        const int fh = ri >= 10, yc = ri - fh * 10;
        const rsrc_t rd = mk(wd + (size_t)(xl * 192 + fh * 96) * (D * 2) + yc * 128, 95 * D * 2 + 128);
        u32x4 wdr[12];
#pragma unroll
        for (int k = 0; k < 12; ++k) {
          wdr[k] = __builtin_amdgcn_raw_buffer_load_b128(rd, r * (12 * D * 2) + c * 16, k * (D * 2), AUX_WD);
          if ((k & 3) == 3) { SB(); __builtin_amdgcn_s_sleep(20); SB(); }
        }
        __builtin_amdgcn_s_sleep(GLU_DELAY);
        u32 hv[2];
        wait1<2, 32>(hv, rws, lane < 48 ? WS_H + (xl * 192 + fh * 48 + lane) * 8 : OOB, 96 * 8, tag);
#pragma unroll
        for (int i = 0; i < 2; ++i) *(u32x2*)&L.hbf[i][lane * 2] = u32x2{(hv[i] & 0xffffu) | (hv[i] << 16), (hv[i] & 0xffff0000u) | (hv[i] >> 16)};
        asm volatile("s_waitcnt lgkmcnt(0)" ::: "memory");
        f32x4 y0 = {0, 0, 0, 0}, y1 = {0, 0, 0, 0};
#pragma unroll
        for (int k4 = 0; k4 < 3; ++k4) {
          const u32x4 h0 = *(const u32x4*)&L.hbf[0][r * 12 + k4 * 4], h1 = *(const u32x4*)&L.hbf[1][r * 12 + k4 * 4];
#pragma unroll
          for (int t = 0; t < 4; ++t) { y0 = mfma(amask(h0[t], mask), wdr[k4 * 4 + t], y0); y1 = mfma(amask(h1[t], mask), wdr[k4 * 4 + t], y1); }
        }
        st2(rw, WS_Y + (((b3 ? 16 : 0) + xl * 2 + fh) * D + yc * 64 + fcol) * 8, fold2(y0, y1, b3), tag);
      }
    }
    if (w == 0 || reducer) {
#pragma unroll 1
      for (u32 spin = 0; spin < SPIN_CAP * 16; ++spin) {
        const u32 f = ((lvu32*)L.ml)[(lane & 3) * 4 + 2];
        if (__all(f != 0u)) break;
        __builtin_amdgcn_s_sleep(4);
      }
      asm volatile("" ::: "memory");
    }
    if (w == 0) {
      const f32x4 mine = L.ml[lane & 3];
      float M = mine[0];
      M = fmaxf(M, dpp<XOR1>(M)); M = fmaxf(M, dpp<XOR2>(M));
      float l = mine[1] * __builtin_amdgcn_exp2f((mine[0] - M) * LOG2E);
      l += dpp<XOR1>(l); l += dpp<XOR2>(l);
      st2(rw, lane == 0 ? WS_ML + wgid * 128 : OOB, f32x2{M, l}, tag);
      __builtin_amdgcn_s_sleep(ML_DELAY);
      f32x2 g[1];
      wait2<1, 32>(g, rws, lane < 4 ? WS_ML + ((wgid & ~3) + lane) * 128 : OOB, 0, tag);
      float MM = g[0][0];
      MM = fmaxf(MM, dpp<XOR1>(MM)); MM = fmaxf(MM, dpp<XOR2>(MM));
      float LL = g[0][1] * __builtin_amdgcn_exp2f((g[0][0] - MM) * LOG2E);
      LL += dpp<XOR1>(LL); LL += dpp<XOR2>(LL);
      if (lane < 4) L.scale[lane] = __builtin_amdgcn_exp2f((mine[0] - MM) * LOG2E) * __builtin_amdgcn_rcpf(LL);
    } else if (reducer) {
      __builtin_amdgcn_s_sleep(ML_DELAY);
      xres = lo2f(__builtin_amdgcn_raw_buffer_load_b16(rx, (rb * D + task * 8 + c) * 2, 0, 0));
      u32 g[2];
      wait1<2, 32>(g, rws, WS_Y + ((rb * 16 + r * 2) * D + task * 8 + c) * 8, D * 8, tag);
      const float yv = rbf(col_sum(__uint_as_float(g[0]) + __uint_as_float(g[1])));
      gate = rbf(yv * __builtin_amdgcn_rcpf(1.f + __builtin_amdgcn_exp2f(-yv * LOG2E)));
    }
    TS(6);
    if (WOA != 1) issue_wo(WOA == 0);
    const u32 po = WS_O + ((xl * 8 + jj) * 768 + hh * 192 + tid) * 8;
    const u32 ps = WS_S + (((b3 ? 24 : 0) + xt * 3 + t3) * D + cb * 64 + fcol) * 8;
    const u32 pr = WS_S + ((rb * 24 + r * 3) * D + task * 8 + c) * 8;
    asm volatile("" :: "v"(po), "v"(ps), "v"(pr));
    barrier();
    TS(7);
    if (w < 3) {
      float op[8];
      const f32x4 sc4 = *(const f32x4*)L.scale;
#pragma unroll
      for (int i = 0; i < 8; ++i) op[i] = L.opart[i][tid];
      SB();
      const float o = (sc4[0] * (op[0] + op[1]) + sc4[1] * (op[2] + op[3])) + (sc4[2] * (op[4] + op[5]) + sc4[3] * (op[6] + op[7]));
      st1(rw, po, __float_as_uint(o), tag);
    }
    TS(8);
    if (w == 3 && lane == 0) { __hip_atomic_store((gu32*)(ws + WS_STAMP + 1024 + blockIdx.x * 4), (u32)__builtin_amdgcn_s_memrealtime(), __ATOMIC_RELAXED, __HIP_MEMORY_SCOPE_AGENT); }
    if (WOA == 1) issue_wo(false);
    if (otask) {
      barrier();
      TS(10);
      out_proj();
      barrier();
      TS(11);
      if (w == 0) {
        f32x2 sp[8];
#pragma unroll
        for (int p = 0; p < 8; ++p) sp[p] = L.sred[p][lane];
        SB();
        st2(rw, ps, ((sp[0] + sp[1]) + (sp[2] + sp[3])) + ((sp[4] + sp[5]) + (sp[6] + sp[7])), tag);
      }
    }
    TS(12);
    if (reducer) {
      u32 g[3];
      wait1<3, TSLEEP>(g, rws, pr, D * 8, tag);
      TS(13);
      const float sv = rbf(col_sum((__uint_as_float(g[0]) + __uint_as_float(g[1])) + __uint_as_float(g[2])));
      const float res = xres + rbf(sv * gate);
      if (lane < 8) ((unsigned short*)out)[rb * D + task * 8 + c] = (unsigned short)dupbf(res);
      if (blockIdx.x == 0 && lane == 0) __hip_atomic_store((gu32*)(ws + WS_EPOCH), tag, __ATOMIC_RELAXED, __HIP_MEMORY_SCOPE_AGENT);
      if (blockIdx.x == 0) {
        const u32 tend = (u32)__builtin_amdgcn_s_memrealtime();
        int mn = 1 << 30, mx = -(1 << 30), omx = -(1 << 30), omn = 1 << 30, qmx = -(1 << 30);
        for (int i = 0; i < 4; ++i) {
          const int a = (int)(__hip_atomic_load((gu32*)(ws + WS_STAMP + (i * 64 + lane) * 4), __ATOMIC_RELAXED, __HIP_MEMORY_SCOPE_AGENT) - lk0);
          const int o = (int)(__hip_atomic_load((gu32*)(ws + WS_STAMP + 1024 + (i * 64 + lane) * 4), __ATOMIC_RELAXED, __HIP_MEMORY_SCOPE_AGENT) - lk0);
          const int qq = (int)(__hip_atomic_load((gu32*)(ws + WS_STAMP + 2048 + (i * 64 + lane) * 4), __ATOMIC_RELAXED, __HIP_MEMORY_SCOPE_AGENT) - lk0);
          mn = min(mn, a); mx = max(mx, a); omx = max(omx, o); omn = min(omn, o); qmx = max(qmx, qq);
        }
        for (int d = 32; d; d >>= 1) { mn = min(mn, __shfl_xor(mn, d)); mx = max(mx, __shfl_xor(mx, d)); omx = max(omx, __shfl_xor(omx, d)); omn = min(omn, __shfl_xor(omn, d)); qmx = max(qmx, __shfl_xor(qmx, d)); }
        const int te = (int)(tend - lk0);
        int dd = 0;
        dd = dd < 0 ? 0 : (dd > 20000 ? 20000 : dd);
        const u32 d = __builtin_amdgcn_readfirstlane(dd) * 1;
        while ((u32)__builtin_amdgcn_s_memrealtime() - tend < d) __builtin_amdgcn_s_sleep(1);
      }
    }
  }
}
}

extern "C" void kernel_launch(void* const* d_in, const int* in_sizes, int n_in, void* d_out, int out_size, void* d_ws, size_t ws_size, hipStream_t stream) {
  fused_decode<<<256, NW * 64, 0, stream>>>((const char*)d_in[0], (const char*)d_in[1], (const char*)d_in[2], (const char*)d_in[3], (const char*)d_in[4],
                                            (const char*)d_in[6], (const char*)d_in[7], (const char*)d_in[8], (char*)d_out, (char*)d_ws);
}
```

```cpp
#include <hip/hip_runtime.h>
#include <hip/hip_bf16.h>
#include <stdint.h>
#include <type_traits>

#define PARK 14
#define RD 5
#define CATCH 4
#define DELAY 48
#define TAU 415
#define T0 1950
#define STAG 11
#define BURST 60
#define ROTX (hh * 8 + b * 5)
#define NA 17
#define NB 31
#define NC 47
#define NA2 35
#define NB2 45
#define NC2 57
#define ROUNDS 1
#define AD_SLEEP 90
#define MARGIN 15
#define KAPPA 20
#define LAMBDA 40
#define SPCAP 180
#define WO_X 40
#define WO_AT 63
#define WO_GAP 13
#define GX 175
#define CODE_BYTES 25088

namespace {
constexpr int D = 640, H = 32, DH = 192, F = 1536;
constexpr int NW = 8;
constexpr float LOG2E = 1.4426950408889634f;
typedef unsigned u32;
typedef unsigned long long u64;
typedef u32 u32x2 __attribute__((ext_vector_type(2)));
typedef u32 u32x4 __attribute__((ext_vector_type(4)));
typedef u64 u64x2 __attribute__((ext_vector_type(2)));
typedef u64 u64x8 __attribute__((ext_vector_type(8)));
typedef float f32x2 __attribute__((ext_vector_type(2)));
typedef float f32x4 __attribute__((ext_vector_type(4)));
typedef short s16x4 __attribute__((ext_vector_type(4)));
typedef __bf16 bf16x2 __attribute__((ext_vector_type(2)));
typedef __bf16 bf16x8 __attribute__((ext_vector_type(8)));
typedef __amdgpu_buffer_rsrc_t rsrc_t;
typedef __attribute__((address_space(1))) u32 gu32;
typedef __attribute__((address_space(3))) volatile u32 lvu32;
typedef __attribute__((address_space(3))) volatile u32x4 lvu32x4;
typedef __attribute__((address_space(3))) s16x4 ls16x4;
#define SB() __builtin_amdgcn_sched_barrier(0)
#define OPQ(v) asm volatile("" : "+s"(v))

constexpr u32 WS_EPOCH = 0;
constexpr u32 WS_Q = 4096;
constexpr u32 WS_GU = WS_Q + 32 * 2 * 2 * 192 * 8;
constexpr u32 WS_H = WS_GU + 8 * 8 * 2 * 192 * 8;
constexpr u32 WS_ML = WS_H + 8 * 192 * 8;
constexpr u32 WS_O = WS_ML + 256 * 16 * 8;
constexpr u32 WS_S = WS_O + 8 * 8 * 768 * 8;
constexpr u32 WS_T = WS_S + 2 * 40 * 640 * 8;
constexpr u32 WS_END = WS_T + 512 * 8;
constexpr u32 WS_STAMP = 4u << 20;
constexpr u32 SPIN_CAP = 1u << 20;
constexpr u32 OOB = 0x7ffffff0u;

__device__ __forceinline__ rsrc_t mk(const void* p, u32 bytes) { return __builtin_amdgcn_make_buffer_rsrc(const_cast<void*>(p), 0, (int)bytes, 0x00020000); }
__device__ __forceinline__ u32x4 mks(const void* p, u32 bytes) { return u32x4{(u32)(uintptr_t)p, (u32)((uintptr_t)p >> 32) & 0xffffu, bytes, 0x00020000u}; }
__device__ __forceinline__ u32 clk() { return (u32)__builtin_amdgcn_s_memrealtime(); }
__device__ __forceinline__ float dot2(u32 a, u32 b, float c) { return __builtin_amdgcn_fdot2_f32_bf16(__builtin_bit_cast(bf16x2, a), __builtin_bit_cast(bf16x2, b), c, false); }
__device__ __forceinline__ f32x4 mfma(u32x4 a, u32x4 b, f32x4 c) { return __builtin_amdgcn_mfma_f32_16x16x32_bf16(__builtin_bit_cast(bf16x8, a), __builtin_bit_cast(bf16x8, b), c, 0, 0, 0); }
template <int C> __device__ __forceinline__ float dpp(float x) { return __uint_as_float((u32)__builtin_amdgcn_mov_dpp((int)__float_as_uint(x), C, 0xf, 0xf, true)); }
constexpr int XOR1 = 0xB1, XOR2 = 0x4E, HMIR = 0x141, ROR8 = 0x128;
__device__ __forceinline__ float lo2f(u32 v) { return __uint_as_float(v << 16); }
__device__ __forceinline__ float hi2f(u32 v) { return __uint_as_float(v & 0xffff0000u); }
__device__ __forceinline__ float rbf(float f) { return (float)(__bf16)f; }
__device__ __forceinline__ u32 pkbf(float a, float b) { bf16x2 v = {(__bf16)a, (__bf16)b}; return __builtin_bit_cast(u32, v); }
__device__ __forceinline__ u32 dupbf(float f) { return pkbf(f, f); }
__device__ __forceinline__ float x16_sum(float x) {
  auto s = __builtin_amdgcn_permlane16_swap(__float_as_uint(x), __float_as_uint(x), false, false);
  return __uint_as_float(s[0]) + __uint_as_float(s[1]);
}
__device__ __forceinline__ float x32_sum(float x) {
  auto t = __builtin_amdgcn_permlane32_swap(__float_as_uint(x), __float_as_uint(x), false, false);
  return __uint_as_float(t[0]) + __uint_as_float(t[1]);
}
__device__ __forceinline__ float x16_32_max(float x) {
  auto s = __builtin_amdgcn_permlane16_swap(__float_as_uint(x), __float_as_uint(x), false, false);
  x = fmaxf(__uint_as_float(s[0]), __uint_as_float(s[1]));
  auto t = __builtin_amdgcn_permlane32_swap(__float_as_uint(x), __float_as_uint(x), false, false);
  return fmaxf(__uint_as_float(t[0]), __uint_as_float(t[1]));
}
__device__ __forceinline__ float half_sum(float x) { x += dpp<XOR1>(x); x += dpp<XOR2>(x); x += dpp<HMIR>(x); x += dpp<ROR8>(x); return x16_sum(x); }
__device__ __forceinline__ float col_sum(float x) { x += dpp<ROR8>(x); return x32_sum(x16_sum(x)); }
__device__ __forceinline__ void barrier() { asm volatile("s_waitcnt lgkmcnt(0)\n\ts_barrier" ::: "memory"); }
struct Acc2 { f32x4 lo = {0, 0, 0, 0}, hi = {0, 0, 0, 0}; };
__device__ __forceinline__ void gemv2(Acc2& acc, u32 dup, const u32 (&m)[2], u32x4 wv) {
  const s16x4 a = __builtin_bit_cast(s16x4, u32x2{dup & m[0], dup & m[1]});
  acc.lo = __builtin_amdgcn_mfma_f32_16x16x16bf16_1k(a, __builtin_bit_cast(s16x4, u32x2{wv[0], wv[1]}), acc.lo, 0, 0, 0);
  acc.hi = __builtin_amdgcn_mfma_f32_16x16x16bf16_1k(a, __builtin_bit_cast(s16x4, u32x2{wv[2], wv[3]}), acc.hi, 0, 0, 0);
}
__device__ __forceinline__ f32x2 fold2(const Acc2& acc, bool b3) {
  float v[4];
#pragma unroll
  for (int e = 0; e < 4; ++e) {
    auto p = __builtin_amdgcn_permlane32_swap(__float_as_uint(acc.lo[e]), __float_as_uint(acc.hi[e]), false, false);
    v[e] = e < 2 ? __uint_as_float(p[0]) + dpp<ROR8>(__uint_as_float(p[1])) : __uint_as_float(p[1]) + dpp<ROR8>(__uint_as_float(p[0]));
  }
  return f32x2{b3 ? v[2] : v[0], b3 ? v[3] : v[1]};
}
__device__ __forceinline__ u32x4 bfrag(const char* p) {
  const u32x2 lo = __builtin_bit_cast(u32x2, __builtin_amdgcn_ds_read_tr16_b64_v4i16((ls16x4*)p));
  const u32x2 hi = __builtin_bit_cast(u32x2, __builtin_amdgcn_ds_read_tr16_b64_v4i16((ls16x4*)(p + 2048)));
  return u32x4{lo[0], lo[1], hi[0], hi[1]};
}
template <int I, int N, typename Fn> __device__ __forceinline__ void static_for(Fn&& f) {
  if constexpr (I < N) { f(std::integral_constant<int, I>{}); static_for<I + 1, N>(f); }
}

__device__ __forceinline__ void st1(rsrc_t rw, u32 off, u32 v, u32 tag) { __builtin_amdgcn_raw_buffer_store_b64(u32x2{v, tag}, rw, off, 0, 16); }
__device__ __forceinline__ void st2(rsrc_t rw, u32 off, f32x2 v, u32 tag) {
  __builtin_amdgcn_raw_buffer_store_b128(u32x4{__float_as_uint(v[0]), tag, __float_as_uint(v[1]), tag}, rw, off, 0, 16);
}
template <int N> __device__ __forceinline__ void ld1(u32x2 (&g)[N], u32x4 rw, const u32 (&o)[N]) {
  if constexpr (N == 2) asm volatile("buffer_load_dwordx2 %0, %2, %4, 0 offen sc1\n\tbuffer_load_dwordx2 %1, %3, %4, 0 offen sc1\n\ts_waitcnt vmcnt(0)"
                                     : "=&v"(g[0]), "=&v"(g[1]) : "v"(o[0]), "v"(o[1]), "s"(rw) : "memory");
  if constexpr (N == 3) asm volatile("buffer_load_dwordx2 %0, %3, %6, 0 offen sc1\n\tbuffer_load_dwordx2 %1, %4, %6, 0 offen sc1\n\tbuffer_load_dwordx2 %2, %5, %6, 0 offen sc1\n\ts_waitcnt vmcnt(0)"
                                     : "=&v"(g[0]), "=&v"(g[1]), "=&v"(g[2]) : "v"(o[0]), "v"(o[1]), "v"(o[2]), "s"(rw) : "memory");
}
template <int N> __device__ __forceinline__ void ld2(u32x4 (&g)[N], u32x4 rw, const u32 (&o)[N]) {
  if constexpr (N == 1) asm volatile("buffer_load_dwordx4 %0, %1, %2, 0 offen sc1\n\ts_waitcnt vmcnt(0)" : "=&v"(g[0]) : "v"(o[0]), "s"(rw) : "memory");
  if constexpr (N == 2) asm volatile("buffer_load_dwordx4 %0, %2, %4, 0 offen sc1\n\tbuffer_load_dwordx4 %1, %3, %4, 0 offen sc1\n\ts_waitcnt vmcnt(0)"
                                     : "=&v"(g[0]), "=&v"(g[1]) : "v"(o[0]), "v"(o[1]), "s"(rw) : "memory");
  if constexpr (N == 4) asm volatile("buffer_load_dwordx4 %0, %4, %8, 0 offen sc1\n\tbuffer_load_dwordx4 %1, %5, %8, 0 offen sc1\n\tbuffer_load_dwordx4 %2, %6, %8, 0 offen sc1\n\t"
                                     "buffer_load_dwordx4 %3, %7, %8, 0 offen sc1\n\ts_waitcnt vmcnt(0)"
                                     : "=&v"(g[0]), "=&v"(g[1]), "=&v"(g[2]), "=&v"(g[3]) : "v"(o[0]), "v"(o[1]), "v"(o[2]), "v"(o[3]), "s"(rw) : "memory");
}
template <int N, int SLEEP> __device__ __forceinline__ void wait1(u32 (&val)[N], u32x4 rw, u32 off, u32 stride, u32 tag) {
  u32 o[N];
#pragma unroll
  for (int i = 0; i < N; ++i) { val[i] = 0u; o[i] = off + i * stride; }
#pragma unroll 1
  for (u32 spin = 0; spin < SPIN_CAP; ++spin) {
    u32x2 g[N];
    ld1<N>(g, rw, o);
    u32 left = OOB;
#pragma unroll
    for (int i = 0; i < N; ++i) { const bool hit = g[i][1] == tag; val[i] = hit ? g[i][0] : val[i]; o[i] = hit ? OOB : o[i]; left = min(left, o[i]); }
    if (__all(left == OOB)) break;
    if (SLEEP) __builtin_amdgcn_s_sleep(SLEEP);
  }
}
template <int N, int SLEEP> __device__ __forceinline__ u32 wait2(f32x2 (&val)[N], u32x4 rw, u32 off, u32 stride, u32 tag) {
  u32 o[N], spin = 0;
#pragma unroll
  for (int i = 0; i < N; ++i) { val[i] = f32x2{0.f, 0.f}; o[i] = off + i * stride; }
#pragma unroll 1
  for (; spin < SPIN_CAP; ++spin) {
    u32x4 g[N];
    ld2<N>(g, rw, o);
    u32 left = OOB;
#pragma unroll
    for (int i = 0; i < N; ++i) {
      const bool hit = (g[i][1] == tag) & (g[i][3] == tag);
      val[i][0] = hit ? __uint_as_float(g[i][0]) : val[i][0]; val[i][1] = hit ? __uint_as_float(g[i][2]) : val[i][1];
      o[i] = hit ? OOB : o[i]; left = min(left, o[i]);
    }
    if (__all(left == OOB)) break;
    if (SLEEP) __builtin_amdgcn_s_sleep(SLEEP);
  }
  return spin;
}

struct Lds {
  u32 wot[(256 + 96) * 32];
  float opart[8][192];
  f32x2 red[4][3][64];
  u32 xd[4][2][128];
  f32x4 ml[4];
  u32 flag[16];
  u32 pace[12];
  float scale[4];
  u32 qbf[96];
  u32 hbf[2][80];
  u32 obf[2][144];
};
__device__ __forceinline__ void lds_set(u32* p, u32 v) { asm volatile("s_waitcnt lgkmcnt(0)" ::: "memory"); *(lvu32*)p = v; }
template <int SLEEP> __device__ __forceinline__ u32x4 lds_wait4(const u32* p) {
  u32x4 f;
#pragma unroll 1
  for (u32 spin = 0; spin < SPIN_CAP * 16; ++spin) {
    f = *(lvu32x4*)p;
    if (__builtin_amdgcn_readfirstlane(f[0] & f[1] & f[2] & f[3] & 1u)) break;
    if (SLEEP) __builtin_amdgcn_s_sleep(SLEEP);
  }
  asm volatile("" ::: "memory");
  return f;
}
template <int SLEEP> __device__ __forceinline__ void lds_wait1(const u32* p) {
#pragma unroll 1
  for (u32 spin = 0; spin < SPIN_CAP * 16; ++spin) {
    if (__builtin_amdgcn_readfirstlane(*(lvu32*)p)) break;
    if (SLEEP) __builtin_amdgcn_s_sleep(SLEEP);
  }
  asm volatile("" ::: "memory");
}

#define TS(i) do { } while (0)

constexpr int issued(int j) {
  const int a = PARK + (j + 1) / CATCH, b = j + 1 + RD, m = a > b ? a : b;
  return m < 64 ? m : 64;
}
constexpr int tnom(int j) { return (T0 + j * TAU) / 10; }

__global__ void __launch_bounds__(NW * 64, 2) fused_decode(const char* x_, const char* K_, const char* V_, const char* wq_, const char* wo_,
    const char* wg_, const char* wu_, const char* wd_, char* out_, char* ws_) {
  __shared__ __attribute__((aligned(16))) Lds L;
  u64x8 ka; u64x2 kb; u64 tb64, pc64;
  asm volatile("s_load_dwordx16 %0, %4, 0x0\n\ts_load_dwordx4 %1, %4, 0x40\n\ts_memrealtime %2\n\ts_getpc_b64 %3"
               : "=&s"(ka), "=&s"(kb), "=&s"(tb64), "=&s"(pc64) : "s"(__builtin_amdgcn_kernarg_segment_ptr()));
  const int tid = threadIdx.x, lane = tid & 63, w = __builtin_amdgcn_readfirstlane(tid >> 6);
  const int xl = blockIdx.x & 7, rk = blockIdx.x >> 3;
  const u32x4 codepf = __builtin_amdgcn_raw_buffer_load_b128(mk((const char*)(pc64 & ~255ull), w == 4 ? CODE_BYTES : 0), lane * 16, rk * 1024, 0);
  SB();
  const int r = lane >> 3, c = lane & 7, g4 = lane >> 4, li = lane & 31;
  const int hh = rk >> 3, h = xl * 4 + hh, jj = rk & 7, b = jj >> 2, s = jj & 3;
  const bool glu = jj >= 6;
  const int kh = jj >= 3, blk = jj - 3 * kh, hq = (xl ^ 4) * 4 + hh;
  constexpr u32 QS = H * DH * 2, GS = F * 2;
  u32 f_q = kh * (320 * QS) + hq * (DH * 2) + blk * 128, f_g = hh * (160 * GS) + xl * 384, r_q = kh * 640, r_g = hh * 320;
  OPQ(f_q); OPQ(f_g); OPQ(r_q); OPQ(r_g);
  u32 foff = (glu ? f_g : f_q) + (w & 3) * (glu ? 40 * GS : 80 * QS);
  u32 fsize = (glu ? D * GS : D * QS) - foff;
  u32 sa = glu ? GS : 3 * QS, sb = glu ? 128 : QS;
  u32 row2 = (glu ? r_g : r_q) + w * (glu ? 80 : 160);
  u32 fvoff = r * (glu ? 5 * GS : 10 * QS) + c * 16;
  u32 xv0 = (lane >> 5) * (D * 2) + li * 16, xv1 = li < 16 ? xv0 : OOB, xv2 = li < 20 ? (lane >> 5) * (D * 2) + li * 8 : OOB;
  OPQ(foff); OPQ(fsize); OPQ(sa); OPQ(sb); OPQ(row2); asm volatile("" : "+v"(fvoff), "+v"(xv0), "+v"(xv1), "+v"(xv2));
  if (tid < 44) ((lvu32*)L.ml)[tid] = 0u;
  asm volatile("s_waitcnt lgkmcnt(0)" : "+s"(ka), "+s"(kb), "+s"(tb64) :: "memory");
  char* const ws = (char*)kb[1];
  const u32 tbase = (u32)tb64;
  auto tasks = [&](bool& otask, int& t3, int& xt, int& cb, bool& ytask, int& fh, int& xy, int& yc) {
    otask = rk < 30;
    t3 = (rk >= 10) + (rk >= 20);
    const int cb0 = rk - t3 * 10, sw0 = (cb0 & 7) == xl;
    xt = xl ^ 4 ^ sw0; cb = cb0 ^ sw0;
    const int ri = hh * 6 + jj;
    ytask = !glu && ri < 20;
    fh = ri >= 10;
    const int yc0 = ri - fh * 10, sw1 = (yc0 & 7) == xl;
    xy = xl ^ sw1; yc = yc0 ^ sw1;
  };
  auto troff = [&](int l, int ct) { const int row = 4 * (l >> 4) + ((l >> 2) & 3); return row * 128 + (((2 * ct + ((l >> 1) & 1)) ^ (row & 6)) * 16 + (l & 1) * 8); };
  auto kposf = [&](int l) { return (l & 48) + ((l >> 1) & 3) * 4 + ((l >> 3) & 1) * 2 + (l & 1); };

  if (w >= 4) {
    const int sw = w - 4;
    u32 mask[4];
#pragma unroll
    for (int t = 0; t < 4; ++t) mask[t] = ((c >> 1) == t) ? ((c & 1) ? 0xffff0000u : 0x0000ffffu) : 0u;
    const u32 koff = (b * H + h) * (4096 * DH * 2) + (s * 4 + sw) * 3072, ksize = 4096 * DH * 2 - (s * 4 + sw) * 3072;
    const rsrc_t rK = mk((const char*)ka[1] + koff, ksize), rV = mk((const char*)ka[2] + koff, ksize);
    const u32 kvoff = r * 384 + c * 16;
    const int rot = ROTX;
    const u32 tb = tbase + sw * STAG - (3 * STAG) / 2;
    const int bsh = (rot & 1) ? -TAU / 20 : TAU / 20;
    int tau16 = TAU * 16 / 10, tprev = 0; u32 ta = 0;
    u32x4 kv[64][3];
    auto issue_kv = [&](int j) {
      if (j == NC || (ROUNDS > 1 && j == NC2)) {
        tprev = j == NC ? tnom(NC - 1) : tprev + (((NC2 - NC) * tau16) >> 4);
        const u32 t63 = __builtin_amdgcn_readfirstlane(*(lvu32*)&L.pace[4]);
        if (t63) tau16 = (int)(t63 - tbase - tprev) * 16 / (64 - j);
      }
      const int tj = j >= NC ? tprev + (((j - ((ROUNDS > 1 && j >= NC2) ? NC2 : NC) + 1) * tau16) >> 4) : tnom(j);
      const u32 tbj = j < BURST ? ((j & 1) ? tb - bsh : tb + bsh) : tb;
      u32 now;
      while ((int)((now = clk()) - tbj) < tj) __builtin_amdgcn_s_sleep(1);
      if (j == NA || j == NA2) ta = now;
      if (j == NB || j == NB2) *(lvu32*)&L.pace[(j == NB ? 0 : 8) + sw] = (now << 16) | ((now - ta) & 0xffffu) | 1u;
      SB();
#pragma unroll
      for (int i = 0; i < 3; ++i) kv[j][i] = __builtin_amdgcn_raw_buffer_load_b128(j < 32 ? rK : rV, kvoff + i * 128, ((j + rot) & 31) * (16 * 3072), 2);
      SB();
    };
    __builtin_amdgcn_s_barrier();
    SB();
    {
      bool otask, ytask; int t3, xt, cb, fh, xy, yc;
      tasks(otask, t3, xt, cb, ytask, fh, xy, yc);
      if (ytask) {
        const u32x4 rds = mks((const char*)ka[7] + (size_t)(xy * 192 + fh * 96 + sw * 24) * (D * 2) + yc * 128, 23u * D * 2 + 128u);
        u32 keep, a = (u32)(uintptr_t)(__attribute__((address_space(3))) u32*)L.wot + 32768 + sw * 3072;
        asm volatile("s_mov_b32 %0, m0\n\t"
                     "s_mov_b32 m0, %1\n\ts_add_u32 %1, %1, 0x400\n\tbuffer_load_dwordx4 %2, %3, 0 offen nt lds\n\t"
                     "s_mov_b32 m0, %1\n\ts_add_u32 %1, %1, 0x400\n\tbuffer_load_dwordx4 %2, %3, %4 offen nt lds\n\t"
                     "s_mov_b32 m0, %1\n\ts_nop 0\n\tbuffer_load_dwordx4 %2, %3, %5 offen nt lds\n\t"
                     "s_mov_b32 m0, %0"
                     : "=&s"(keep), "+s"(a) : "v"(r * (D * 2) + (c ^ (r & 6)) * 16), "s"(rds), "s"(8 * D * 2), "s"(16 * D * 2) : "memory", "scc");
      }
    }
#pragma unroll
    for (int j = 0; j < PARK; ++j) {
      if (j >= RD) { asm volatile("" :: "v"(kv[j < RD ? 0 : j - RD][0]), "v"(kv[j < RD ? 0 : j - RD][1]), "v"(kv[j < RD ? 0 : j - RD][2])); SB(); }
      issue_kv(j);
    }
    asm volatile("" :: "v"(codepf));
    TS(1);
    const u32 tag = __builtin_amdgcn_readfirstlane(lds_wait4<1>(L.flag)[0]);
    u32x4 q[3];
#pragma unroll
    for (int i = 0; i < 3; ++i) q[i] = *(const u32x4*)&L.qbf[i * 32 + c * 4];
    TS(4);
    f32x4 oacc[3] = {{0, 0, 0, 0}, {0, 0, 0, 0}, {0, 0, 0, 0}};
    float sc[32];
    u32 pd[32];
    float mw = -INFINITY, lw = 0.f, mneg = 0.f;
    static_for<0, 64>([&](auto J) {
      constexpr int j = J;
      if constexpr (j < 32) {
        float a0 = 0.f, a1 = 0.f, a2 = 0.f;
#pragma unroll
        for (int t = 0; t < 4; ++t) { a0 = dot2(kv[j][0][t], q[0][t], a0); a1 = dot2(kv[j][1][t], q[1][t], a1); a2 = dot2(kv[j][2][t], q[2][t], a2); }
        float a = (a0 + a1) + a2;
        a += dpp<XOR1>(a); a += dpp<XOR2>(a); a += dpp<HMIR>(a);
        a = rbf(rbf(a) * 0.07216878364870322f);
        asm volatile("" : "+v"(a));
        sc[j] = a; mw = fmaxf(mw, a);
      } else {
        const u32 p = pd[j - 32];
        const u32x4 a = {p & mask[0], p & mask[1], p & mask[2], p & mask[3]};
#pragma unroll
        for (int i = 0; i < 3; ++i) oacc[i] = mfma(a, kv[j][i], oacc[i]);
      }
      SB();
#pragma unroll
      for (int n = (j ? issued(j - 1) : PARK); n < issued(j); ++n) issue_kv(n);
      if constexpr (j == 57) TS(3);
      if constexpr (j == WO_AT - RD) *(lvu32*)&L.flag[4 + sw] = 1u;
      if constexpr (j == 31) {
        TS(5);
        mw = fmaxf(mw, dpp<ROR8>(mw)); mw = x16_32_max(mw);
        mneg = -mw * LOG2E;
      }
      if constexpr (j >= 31 && j < 35) {
#pragma unroll
        for (int g = (j - 31) * 8; g < (j - 31) * 8 + 8; ++g) { const float p = __builtin_amdgcn_exp2f(fmaf(sc[g], LOG2E, mneg)); lw += p; pd[g] = dupbf(p); asm volatile("" : "+v"(pd[g])); }
        if constexpr (j == 34) {
          lw = col_sum(lw);
          if (lane == 0) *(lvu32x4*)&L.ml[sw] = u32x4{__float_as_uint(mw), __float_as_uint(lw), 1u, 0u};
        }
        SB();
      }
    });
    TS(6);
    if (((lane >> 5) & 1) == ((lane >> 3) & 1)) {
#pragma unroll
      for (int i = 0; i < 3; ++i) *(f32x4*)&L.opart[2 * sw + (lane >> 5)][i * 64 + 8 * c + 4 * (g4 & 1)] = oacc[i];
    }
    lds_set(&L.flag[12 + sw], 1u);
    TS(7);
    int l2 = lane; asm volatile("" : "+v"(l2));
    bool otask, ytask; int t3, xt, cb, fh, xy, yc;
    tasks(otask, t3, xt, cb, ytask, fh, xy, yc);
    if (otask) {
      const u32x4 rws = mks(ws, WS_END);
      const u32 ooff = WS_O + ((xt * 2 + (sw >> 1)) * 4 * 768 + t3 * 256 + (sw & 1) * 128 + lane * 2) * 8;
      const int kpos = kposf(l2);
      asm volatile("" :: "v"(ooff), "v"(kpos));
      u32 tw = __builtin_amdgcn_readfirstlane(*(lvu32*)&L.pace[4]);
      if (!tw) tw = tbase + tnom(63);
      while ((int)(clk() - tw) < GX) __builtin_amdgcn_s_sleep(1);
      TS(8);
      f32x2 g[4];
      wait2<4, 0>(g, rws, ooff, 768 * 8, tag);
      TS(9);
      const f32x2 o = (g[0] + g[1]) + (g[2] + g[3]);
      L.obf[sw >> 1][(sw & 1) * 64 + kpos] = pkbf(o[0], o[1]);
      barrier();
    }
  } else {
    u64 fp = glu ? (jj == 6 ? ka[5] : ka[6]) : ka[3];
    const rsrc_t rf = mk((const char*)fp + foff, fsize);
    const rsrc_t rx = mk((const char*)ka[0], 2 * D * 2);
    u32x4 wr[15], xa, xb, xc; u32x2 xs; u32 epoch;
#pragma unroll
    for (int n = 0; n < 10; ++n) {
      wr[n] = __builtin_amdgcn_raw_buffer_load_b128(rf, fvoff, (n / 3) * sa + (n % 3) * sb, 2); SB();
      if (n == 2) {
        xa = __builtin_amdgcn_raw_buffer_load_b128(rx, xv0, 0, 0);
        xb = __builtin_amdgcn_raw_buffer_load_b128(rx, xv0, 512, 0);
        xc = __builtin_amdgcn_raw_buffer_load_b128(rx, xv1, 1024, 0);
        xs = __builtin_amdgcn_raw_buffer_load_b64(rx, xv2, row2, 0);
        epoch = __hip_atomic_load((gu32*)(ws + WS_EPOCH), __ATOMIC_RELAXED, __HIP_MEMORY_SCOPE_AGENT);
        SB();
      }
    }
    if (glu) {
#pragma unroll
      for (int n = 10; n < 15; ++n) wr[n] = __builtin_amdgcn_raw_buffer_load_b128(rf, fvoff, (n / 3) * GS + (n % 3) * 128, 2);
      SB();
    }
    __builtin_amdgcn_s_barrier();
    SB();
    TS(2);
    const rsrc_t rw = mk(ws, WS_END);
    const u32x4 rws = mks(ws, WS_END);
    const bool b3 = lane & 8;
    const int fcol = 8 * c + 4 * (lane >> 5) + 2 * ((lane >> 3) & 1), fb = g4 & 1;
    const u32 hm = (c & 1) ? 0xffff0000u : 0x0000ffffu;
    const u32 mask2[2] = {(c & 2) ? 0u : hm, (c & 2) ? hm : 0u};
    const u32 tag = (__builtin_amdgcn_readfirstlane(epoch) + 0x9E3779B8u) | 1u;
    {
      float sum = 0.f;
#pragma unroll
      for (int t = 0; t < 4; ++t) { sum = dot2(xa[t], 0x3f803f80u, sum); sum = dot2(xb[t], 0x3f803f80u, sum); sum = dot2(xc[t], 0x3f803f80u, sum); }
      const float mean = half_sum(sum) * (1.f / D);
      float qa = 0.f, qb = 0.f, qc = 0.f;
#pragma unroll
      for (int t = 0; t < 4; ++t) {
        const float d0 = lo2f(xa[t]) - mean, d1 = hi2f(xa[t]) - mean, d2 = lo2f(xb[t]) - mean, d3 = hi2f(xb[t]) - mean, d4 = lo2f(xc[t]) - mean, d5 = hi2f(xc[t]) - mean;
        qa = fmaf(d0, d0, qa); qa = fmaf(d1, d1, qa); qb = fmaf(d2, d2, qb); qb = fmaf(d3, d3, qb); qc = fmaf(d4, d4, qc); qc = fmaf(d5, d5, qc);
      }
      const float var = half_sum(qa + qb + (li < 16 ? qc : 0.f)) * (1.f / D);
      const float mb = rbf(mean);
      const float rs = __builtin_amdgcn_rcpf(rbf(__builtin_amdgcn_sqrtf(rbf(rbf(var) + 1e-5f))));
      *(u32x4*)&L.xd[w][lane >> 5][li * 4] = u32x4{dupbf(rbf(lo2f(xs[0]) - mb) * rs), dupbf(rbf(hi2f(xs[0]) - mb) * rs), dupbf(rbf(lo2f(xs[1]) - mb) * rs), dupbf(rbf(hi2f(xs[1]) - mb) * rs)};
      asm volatile("s_waitcnt lgkmcnt(0)" ::: "memory");
      const u32* xr = &L.xd[w][c >> 2][r * (glu ? 5 : 10)];
      if (glu) {
        Acc2 acc[3];
#pragma unroll
        for (int g = 0; g < 5; ++g) {
          const u32 xv = xr[g];
#pragma unroll
          for (int i = 0; i < 3; ++i) gemv2(acc[i], xv, mask2, wr[g * 3 + i]);
        }
#pragma unroll
        for (int i = 0; i < 3; ++i) L.red[w][i][lane] = fold2(acc[i], b3);
      } else {
        u32x2 xv[5];
#pragma unroll
        for (int g = 0; g < 5; ++g) xv[g] = *(const u32x2*)(xr + g * 2);
        SB();
        Acc2 acc;
#pragma unroll
        for (int g = 0; g < 10; ++g) gemv2(acc, xv[g >> 1][g & 1], mask2, wr[g]);
        L.red[w][0][lane] = fold2(acc, b3);
      }
      lds_set(&L.flag[8 + w], 1u);
      if (w == 0) {
        *(lvu32*)&L.flag[0] = tag;
        lds_wait4<1>(&L.flag[8]);
        const u32 po = glu ? WS_GU + ((((xl * 2 + jj - 6) * 4 + hh) * 2 + fb) * 192 + fcol) * 8
                           : WS_Q + (((hq * 2 + fb) * 2 + kh) * 192 + blk * 64 + fcol) * 8;
        st2(rw, po, (L.red[0][0][lane] + L.red[1][0][lane]) + (L.red[2][0][lane] + L.red[3][0][lane]), tag);
        if (glu) {
#pragma unroll
          for (int i = 1; i < 3; ++i) st2(rw, po + i * 512, (L.red[0][i][lane] + L.red[1][i][lane]) + (L.red[2][i][lane] + L.red[3][i][lane]), tag);
        }
      }
    }
    TS(3);
    if (w != 0) {
      if (!glu) __builtin_amdgcn_s_sleep(DELAY); else __builtin_amdgcn_s_sleep(DELAY / 2);
      f32x2 g[1];
      wait2<1, 8>(g, rws, WS_Q + (((h * 2 + b) * 2 + (lane >> 5)) * 192 + (w - 1) * 64 + li * 2) * 8, 0, tag);
      L.qbf[(w - 1) * 32 + li] = pkbf(x32_sum(g[0][0]), x32_sum(g[0][1]));
      lds_set(&L.flag[w], 1u);
    }
    TS(5);
    int l2 = lane; asm volatile("" : "+v"(l2));
    bool otask, ytask; int t3, xt, cb, fh, xy, yc;
    tasks(otask, t3, xt, cb, ytask, fh, xy, yc);
    const int wgid = (b * H + h) * 4 + s;
    if (w == 0) {
#pragma unroll 1
      for (u32 spin = 0; spin < SPIN_CAP * 16; ++spin) {
        const u32 f = ((lvu32*)L.ml)[(lane & 3) * 4 + 2];
        if (__all(f != 0u)) break;
        __builtin_amdgcn_s_sleep(4);
      }
      asm volatile("" ::: "memory");
      const f32x4 mine = L.ml[lane & 3];
      float M = mine[0];
      M = fmaxf(M, dpp<XOR1>(M)); M = fmaxf(M, dpp<XOR2>(M));
      float l = mine[1] * __builtin_amdgcn_exp2f((mine[0] - M) * LOG2E);
      l += dpp<XOR1>(l); l += dpp<XOR2>(l);
      st2(rw, lane == 0 ? WS_ML + wgid * 128 : OOB, f32x2{M, l}, tag);
      __builtin_amdgcn_s_sleep(127);
      f32x2 g[1];
      wait2<1, 32>(g, rws, lane < 4 ? WS_ML + ((wgid & ~3) + lane) * 128 : OOB, 0, tag);
      float MM = g[0][0];
      MM = fmaxf(MM, dpp<XOR1>(MM)); MM = fmaxf(MM, dpp<XOR2>(MM));
      float LL = g[0][1] * __builtin_amdgcn_exp2f((g[0][0] - MM) * LOG2E);
      LL += dpp<XOR1>(LL); LL += dpp<XOR2>(LL);
      if (lane < 4) L.scale[lane] = __builtin_amdgcn_exp2f((mine[0] - MM) * LOG2E) * __builtin_amdgcn_rcpf(LL);
      lds_set(&L.pace[6], 1u);
    } else if (w == 1) {
      const int hb = hh * 6 + jj - 20;
      __builtin_amdgcn_s_sleep(127);
      if (ytask) {
        lds_wait4<8>(L.pace);
        u32 hv[2];
        wait1<2, 32>(hv, rws, lane < 48 ? WS_H + (xy * 192 + fh * 48 + lane) * 8 : OOB, 96 * 8, tag);
        L.hbf[0][kposf(l2)] = hv[0]; L.hbf[1][kposf(l2)] = hv[1];
        asm volatile("s_waitcnt lgkmcnt(0)" ::: "memory");
        const u32* hp = &L.hbf[(lane >> 2) & 1][g4 * 4];
#pragma unroll 1
        for (int ct = 0; ct < 4; ++ct) {
          f32x4 y = {0, 0, 0, 0};
#pragma unroll
          for (int ks = 0; ks < 3; ++ks) y = mfma(*(const u32x4*)(hp + ks * 16), bfrag((const char*)L.wot + 32768 + troff(l2, ct) + ks * 4096), y);
          st1(rw, lane < 32 ? WS_S + (((lane >> 4) * 40 + xy * 5 + 3 + fh) * D + yc * 64 + ct * 16 + (lane & 15)) * 8 : OOB, __float_as_uint(y[0]), tag);
        }
      } else if (!glu && hb < 3) {
        const u32 go = WS_GU + ((xl * 16 + (lane >> 5)) * 192 + hb * 64 + li * 2) * 8;
        f32x2 g[4], u[4];
        wait2<4, 32>(g, rws, go, 2 * 192 * 8, tag);
        wait2<4, 32>(u, rws, go + 8 * 192 * 8, 2 * 192 * 8, tag);
        const f32x2 gs = (g[0] + g[1]) + (g[2] + g[3]), us = (u[0] + u[1]) + (u[2] + u[3]);
        st1(rw, WS_H + ((xl * 2 + (lane >> 5)) * 96 + hb * 32 + li) * 8, pkbf(rbf(gs[0]) * rbf(us[0]), rbf(gs[1]) * rbf(us[1])), tag);
      }
    } else if (w == 2) {
      u32 tcur = tbase + tnom(63) - 60;
#pragma unroll 1
      for (int rd = 0; rd < ROUNDS; ++rd) {
        const float span = rd ? NB2 - NA2 : NB - NA, left = rd ? 63 - NB2 : 63 - NB;
        const u32x4 p = lds_wait4<8>(&L.pace[rd * 8]);
        const u32 p0 = p[0] >> 16;
        const int db = ((int)(short)((p[1] >> 16) - p0) + (int)(short)((p[2] >> 16) - p0) + (int)(short)((p[3] >> 16) - p0)) >> 2;
        const u32 dab = ((p[0] & 0xffffu) + (p[1] & 0xffffu) + (p[2] & 0xffffu) + (p[3] & 0xffffu)) >> 2;
        st1(rw, lane == 0 ? WS_T + (rd * 256 + xl * 32 + rk) * 8 : OOB, ((p0 + db) << 16) | dab, tag);
        __builtin_amdgcn_s_sleep(AD_SLEEP);
        u32x4 g[2]; const u32 o[2] = {WS_T + rd * 2048 + lane * 16, WS_T + rd * 2048 + 1024 + lane * 16};
        ld2<2>(g, rws, o);
        const u32 now = clk();
        float sb_ = 0.f, sd = 0.f, mxb = -1e9f;
#pragma unroll
        for (int i = 0; i < 4; ++i) {
          const u32 v = g[i >> 1][(i & 1) * 2]; const bool ok = g[i >> 1][(i & 1) * 2 + 1] == tag;
          const float tbv = ok ? (float)(short)((v >> 16) - now) : 0.f;
          sb_ += tbv; mxb = fmaxf(mxb, tbv);
          sd += ok ? (float)(v & 0xffffu) : span * (TAU / 10.f);
        }
        sb_ = x32_sum(half_sum(sb_)) * (1.f / 256);
        sd = x32_sum(half_sum(sd)) * (left / span / 256);
        mxb = fmaxf(mxb, dpp<XOR1>(mxb)); mxb = fmaxf(mxb, dpp<XOR2>(mxb)); mxb = fmaxf(mxb, dpp<HMIR>(mxb)); mxb = fmaxf(mxb, dpp<ROR8>(mxb)); mxb = x16_32_max(mxb);
        const float spread = fminf(mxb - sb_, (float)SPCAP), slow = fmaxf(sd - left * (TAU / 10.f), 0.f);
        const int est = (int)(sb_ + sd + spread * (KAPPA / 100.f) + slow * (LAMBDA / 100.f)) + MARGIN;
        tcur = now + min(max(est, (int)(tcur - now)), (int)(tbase - now) + tnom(63) + 500);
        *(lvu32*)&L.pace[4] = tcur | 1u;
      }
    }
    const bool reducer = w == 3 && (rk < 16 || xl < 2);
    const int task = (xl + (rk & 16) / 2) * 8 + (rk & 7), rb = (rk >> 3) & 1;
    const u32 po = WS_O + ((xl * 8 + jj) * 768 + hh * 192 + tid) * 8;
    const u32 ps = lane < 32 ? WS_S + (((lane >> 4) * 40 + xt * 5 + t3) * D + cb * 64 + w * 16 + (lane & 15)) * 8 : OOB;
    const u32 pr = WS_S + ((rb * 40 + r * 5) * D + task * 8 + c) * 8;
    const u32 pout = lane < 8 ? (rb * D + task * 8 + c) * 2 : OOB;
    const char* tb = (const char*)L.wot + troff(l2, w);
    const u32* ap = &L.obf[(lane >> 2) & 1][g4 * 4];
    float xres = 0.f, gate = 0.f;
    asm volatile("" :: "v"(po), "v"(ps), "v"(pr), "v"(pout));
    if (w == 3) {
      if (reducer) {
        lds_wait1<8>(&L.pace[6]);
        xres = lo2f(__builtin_amdgcn_raw_buffer_load_b16(rx, pout, 0, 0));
        u32 g[2];
        wait1<2, 32>(g, rws, pr + 3 * D * 8, D * 8, tag);
        const float yv = rbf(col_sum(__uint_as_float(g[0]) + __uint_as_float(g[1])));
        gate = rbf(yv * __builtin_amdgcn_rcpf(1.f + __builtin_amdgcn_exp2f(-yv * LOG2E)));
        asm volatile("" : "+v"(xres), "+v"(gate));
      }
      const u32x4 ros = mks((const char*)ka[4] + (size_t)(xt * 768 + t3 * 256) * (D * 2) + (otask ? cb * 128 : 0), otask ? 255u * D * 2 + 128u : 0u);
      const u32 wvoff = r * (D * 2) + (c ^ (r & 6)) * 16;
      const u32 wlds = (u32)(uintptr_t)(__attribute__((address_space(3))) u32*)L.wot;
      auto dma4 = [&](u32x4 rs, u32 a, u32 o) {
        u32 keep;
        asm volatile("s_mov_b32 %0, m0\n\t"
                     "s_mov_b32 m0, %1\n\ts_add_u32 %1, %1, 0x400\n\tbuffer_load_dwordx4 %3, %4, %2 offen nt lds\n\ts_add_u32 %2, %2, 0x2800\n\t"
                     "s_mov_b32 m0, %1\n\ts_add_u32 %1, %1, 0x400\n\tbuffer_load_dwordx4 %3, %4, %2 offen nt lds\n\ts_add_u32 %2, %2, 0x2800\n\t"
                     "s_mov_b32 m0, %1\n\ts_add_u32 %1, %1, 0x400\n\tbuffer_load_dwordx4 %3, %4, %2 offen nt lds\n\ts_add_u32 %2, %2, 0x2800\n\t"
                     "s_mov_b32 m0, %1\n\ts_nop 0\n\tbuffer_load_dwordx4 %3, %4, %2 offen nt lds\n\t"
                     "s_mov_b32 m0, %0"
                     : "=&s"(keep), "+s"(a), "+s"(o) : "v"(wvoff), "s"(rs) : "memory", "scc");
      };
      lds_wait4<1>(&L.flag[4]);
      u32 tw = __builtin_amdgcn_readfirstlane(*(lvu32*)&L.pace[4]);
      if (!tw) tw = tbase + tnom(63);
      TS(4);
#pragma unroll 1
      for (int k = 0; k < 8; ++k) {
        while ((int)(clk() - tw) < WO_X + k * WO_GAP) __builtin_amdgcn_s_sleep(1);
        dma4(ros, wlds + k * 4096, k * (32 * D * 2));
      }
      TS(6);
      asm volatile("s_waitcnt vmcnt(0)" ::: "memory");
      *(lvu32*)&L.pace[5] = 1u;
      TS(1);
    } else {
      lds_wait1<4>(&L.pace[6]);
      f32x4 sc4 = *(const f32x4*)L.scale;
      asm volatile("" : "+v"(sc4));
      lds_wait4<2>(&L.flag[4]);
      lds_wait4<0>(&L.flag[12]);
      TS(7);
      float op[8];
#pragma unroll
      for (int i = 0; i < 8; ++i) op[i] = L.opart[i][tid];
      SB();
      const float o = (sc4[0] * (op[0] + op[1]) + sc4[1] * (op[2] + op[3])) + (sc4[2] * (op[4] + op[5]) + sc4[3] * (op[6] + op[7]));
      st1(rw, po, __float_as_uint(o), tag);
      TS(8);
    }
    if (otask) {
      lds_wait1<0>(&L.pace[5]);
      u32x4 bf[8];
#pragma unroll
      for (int ks = 0; ks < 8; ++ks) bf[ks] = bfrag(tb + ks * 4096);
      TS(9);
      barrier();
      TS(13);
      u32x4 af[8];
#pragma unroll
      for (int ks = 0; ks < 8; ++ks) af[ks] = *(const u32x4*)(ap + ks * 16);
      SB();
      f32x4 a0 = {0, 0, 0, 0}, a1 = {0, 0, 0, 0};
#pragma unroll
      for (int ks = 0; ks < 8; ks += 2) { a0 = mfma(af[ks], bf[ks], a0); a1 = mfma(af[ks + 1], bf[ks + 1], a1); }
      st1(rw, ps, __float_as_uint(a0[0] + a1[0]), tag);
    }
    TS(12);
    if (reducer) {
      asm volatile("s_waitcnt vmcnt(0)" ::: "memory");
      u32 g[3];
      wait1<3, 0>(g, rws, pr, D * 8, tag);
      const float sv = rbf(col_sum((__uint_as_float(g[0]) + __uint_as_float(g[1])) + __uint_as_float(g[2])));
      __builtin_amdgcn_raw_buffer_store_b16((unsigned short)dupbf(xres + rbf(sv * gate)), mk((char*)kb[0], 2 * D * 2), pout, 0, 0);
      if (blockIdx.x == 0 && lane == 0) __hip_atomic_store((gu32*)(ws + WS_EPOCH), tag, __ATOMIC_RELAXED, __HIP_MEMORY_SCOPE_AGENT);
    }
  }
}
}

extern "C" void kernel_launch(void* const* d_in, const int* in_sizes, int n_in, void* d_out, int out_size, void* d_ws, size_t ws_size, hipStream_t stream) {
  fused_decode<<<256, NW * 64, 0, stream>>>((const char*)d_in[0], (const char*)d_in[1], (const char*)d_in[2], (const char*)d_in[3], (const char*)d_in[4],
                                            (const char*)d_in[6], (const char*)d_in[7], (const char*)d_in[8], (char*)d_out, (char*)d_ws);
}
```
